```python
import torch

def _softmax(x, axis=-1):
    return torch.softmax(x, dim=axis)
def _gelu(x):
    return torch.nn.functional.gelu(x, approximate="tanh")
def _silu(x):
    return torch.nn.functional.silu(x)
def _sigmoid(x):
    return torch.sigmoid(x)
def _layernorm(x, axis=-1, eps=1e-5):
    m = x.mean(dim=axis, keepdim=True)
    v = x.var(dim=axis, keepdim=True, unbiased=False)
    return (x - m) / torch.sqrt(v + eps)
def _scatter_add(tgt, idx, src, axis=0):
    return tgt.clone().index_add_(axis, idx, src)
def _segment_sum(src, seg, n):
    out = torch.zeros((n,) + src.shape[1:], dtype=src.dtype)
    return out.index_add_(0, seg, src)
def _pad(x, pw):
    flat = tuple(v for pair in reversed(pw) for v in pair)
    return torch.nn.functional.pad(x, flat)

kernel_name = 'standalone__gqa_block_cached__near_ridge'

def setup_inputs():

    _w = (lambda a: (lambda t: t.to(torch.bfloat16) if t.is_floating_point() else t)(torch.as_tensor(a)))
    B, Sq, D = 8, 32, 512
    Hq, Hkv, Sk, Dh = 32, 8, 2048, 128
    assert Hq % Hkv == 0, (Hq, Hkv)
    return {
        "x": _w((lambda *s: torch.randn(*s, dtype=torch.bfloat16) * 0.8)(B, Sq, D)),
        "k_cache": _w((lambda *s: torch.randn(*s, dtype=torch.bfloat16) * 0.8)(B, Hkv, Sk, Dh)),
        "v_cache": _w((lambda *s: torch.randn(*s, dtype=torch.bfloat16) * 0.05)(B, Hkv, Sk, Dh)),
        "wq": _w((lambda *s: torch.randn(*s, dtype=torch.bfloat16) * 0.8)(D, Hq * Dh) * (D ** -0.5)),
        "wo": _w((lambda *s: torch.randn(*s, dtype=torch.bfloat16) * 0.05)(Hq * Dh, D) * ((Hq * Dh) ** -0.5)),
    }

def reference(x, k_cache, v_cache, wq, wo):
    B, Sq, D = x.shape
    Hq, Dh = 32, 128
    Hkv = k_cache.shape[1]
    rep = Hq // Hkv
    q = (lambda x, *d: x.permute(*d))((x @ wq).reshape(B, Sq, Hq, Dh), 0, 2, 1, 3)
    k = (lambda x, n, axis: torch.repeat_interleave(x, n, dim=axis))(k_cache, rep, axis=1)
    v = (lambda x, n, axis: torch.repeat_interleave(x, n, dim=axis))(v_cache, rep, axis=1)
    scores = torch.einsum("bhqd,bhkd->bhqk", q, k) * (Dh ** -0.5)
    p = _softmax(scores, axis=-1)
    o = torch.einsum("bhqk,bhkd->bhqd", p, v)
    return (lambda x, *d: x.permute(*d))(o, 0, 2, 1, 3).reshape(B, Sq, Hq * Dh) @ wo
```

```cpp
#include <hip/hip_runtime.h>
#include <hip/hip_bf16.h>
#include <stdint.h>

typedef __attribute__((ext_vector_type(8))) __bf16 bf16x8;
typedef __attribute__((ext_vector_type(4))) __bf16 bf16x4;
typedef __attribute__((ext_vector_type(2))) __bf16 bf16x2;
typedef __attribute__((ext_vector_type(16))) float f32x16;
typedef __attribute__((ext_vector_type(4))) float f32x4;
typedef __attribute__((ext_vector_type(2))) float f32x2;
typedef __attribute__((ext_vector_type(4))) unsigned u32x4;
typedef __attribute__((ext_vector_type(2))) unsigned u32x2;
#define LAS __attribute__((address_space(3)))
#define DI __device__ __forceinline__

constexpr int NB = 8, SQ = 32, DM = 512, HQ = 32, HKV = 8, SK = 2048, DH = 128;
constexpr float QSCALE = 0.12751743082f;

constexpr size_t WS_Q = 0;
constexpr size_t WS_PART = WS_Q + 256 * 8192;
constexpr size_t WS_ML = WS_PART + 1024 * 8192;
constexpr size_t WS_PO = WS_ML + 1024 * 256;
constexpr size_t WS_END = WS_PO + 256 * 16384;

DI unsigned cvtpk(float lo, float hi) { f32x2 v = {lo, hi}; bf16x2 b = __builtin_convertvector(v, bf16x2); return __builtin_bit_cast(unsigned, b); }
DI bf16x8 asbf(u32x4 v) { return __builtin_bit_cast(bf16x8, v); }
DI int rfl(int x) { return __builtin_amdgcn_readfirstlane(x); }
#define SBAR() __builtin_amdgcn_sched_barrier(0)
#define BAR() do { SBAR(); asm volatile("s_waitcnt lgkmcnt(0)\n\ts_barrier" ::: "memory"); SBAR(); } while (0)
#define WAITV(n) asm volatile("s_waitcnt vmcnt(%0)" ::"n"(n) : "memory")
#define WAITL() asm volatile("s_waitcnt lgkmcnt(0)" ::: "memory")

DI void q_phase(const unsigned short* __restrict__ x, const unsigned short* __restrict__ wq, char* lds_scr, char* qout, int b, int h, int w, int lane) {
  const int c = lane & 15, g = lane >> 4;
  f32x4 acc[2][8];
#pragma unroll
  for (int rh = 0; rh < 2; ++rh)
#pragma unroll
    for (int jj = 0; jj < 8; ++jj) acc[rh][jj] = f32x4{0.f, 0.f, 0.f, 0.f};
  const unsigned short* wp = wq + (size_t)(128 * w + 8 * g) * 4096 + h * 128 + 8 * c;
  const unsigned short* xp = x + (size_t)(b * 32 + c) * 512 + 128 * w + 8 * g;
  u32x4 wr[4][8]; u32x4 xa[4][2];
#pragma unroll
  for (int st = 0; st < 4; ++st) {
#pragma unroll
    for (int i = 0; i < 8; ++i) wr[st][i] = *(const u32x4*)(wp + (size_t)(32 * st + i) * 4096);
#pragma unroll
    for (int rh = 0; rh < 2; ++rh) xa[st][rh] = *(const u32x4*)(xp + rh * 16 * 512 + 32 * st);
  }
#pragma unroll
  for (int st = 0; st < 4; ++st) {
#pragma unroll
    for (int jj = 0; jj < 8; ++jj) {
      u32x4 a;
#pragma unroll
      for (int e = 0; e < 4; ++e) a[e] = __builtin_amdgcn_perm(wr[st][2 * e + 1][jj >> 1], wr[st][2 * e][jj >> 1], (jj & 1) ? 0x07060302u : 0x05040100u);
#pragma unroll
      for (int rh = 0; rh < 2; ++rh) acc[rh][jj] = __builtin_amdgcn_mfma_f32_16x16x32_bf16(asbf(a), asbf(xa[st][rh]), acc[rh][jj], 0, 0, 0);
    }
  }
#pragma unroll
  for (int reg = 0; reg < 4; ++reg) {
    if (reg != w) {
      const int slot = reg * 3 + (w < reg ? w : w - 1);
#pragma unroll
      for (int rh = 0; rh < 2; ++rh)
#pragma unroll
        for (int hf = 0; hf < 2; ++hf)
          *(f32x4*)(lds_scr + ((slot * 2 + rh) * 2 + hf) * 1024 + lane * 16) = f32x4{acc[rh][4 * hf][reg], acc[rh][4 * hf + 1][reg], acc[rh][4 * hf + 2][reg], acc[rh][4 * hf + 3][reg]};
    }
  }
  f32x4 own[2][2];
#pragma unroll
  for (int rh = 0; rh < 2; ++rh)
#pragma unroll
    for (int hf = 0; hf < 2; ++hf) {
      f32x4 v;
#pragma unroll
      for (int e = 0; e < 4; ++e) {
        const float a0 = acc[rh][4 * hf + e][0], a1 = acc[rh][4 * hf + e][1], a2 = acc[rh][4 * hf + e][2], a3 = acc[rh][4 * hf + e][3];
        v[e] = w == 0 ? a0 : (w == 1 ? a1 : (w == 2 ? a2 : a3));
      }
      own[rh][hf] = v;
    }
  WAITL();
  BAR();
#pragma unroll
  for (int j = 0; j < 3; ++j)
#pragma unroll
    for (int rh = 0; rh < 2; ++rh)
#pragma unroll
      for (int hf = 0; hf < 2; ++hf) own[rh][hf] += *(const f32x4*)(lds_scr + (((w * 3 + j) * 2 + rh) * 2 + hf) * 1024 + lane * 16);
#pragma unroll
  for (int rh = 0; rh < 2; ++rh) {
    u32x4 o;
    o[0] = cvtpk(own[rh][0][0] * QSCALE, own[rh][0][1] * QSCALE);
    o[1] = cvtpk(own[rh][0][2] * QSCALE, own[rh][0][3] * QSCALE);
    o[2] = cvtpk(own[rh][1][0] * QSCALE, own[rh][1][1] * QSCALE);
    o[3] = cvtpk(own[rh][1][2] * QSCALE, own[rh][1][3] * QSCALE);
    *(u32x4*)(qout + ((2 * g + (w >> 1)) * 64 + (w & 1) * 32 + 16 * rh + c) * 16) = o;
  }
}

__global__ __launch_bounds__(256) void k_q(const unsigned short* __restrict__ x, const unsigned short* __restrict__ wq, char* __restrict__ ws) {
  extern __shared__ __attribute__((aligned(16))) char lds[];
  const int bid = blockIdx.x, hkv = bid & 7, b = (bid >> 3) & 7, sp = bid >> 6;
  const int w = rfl(threadIdx.x >> 6), lane = threadIdx.x & 63;
  const int h = hkv * 4 + sp;
  q_phase(x, wq, lds, ws + WS_Q + (size_t)(b * 32 + h) * 8192, b, h, w, lane);
}

constexpr unsigned SLOT = 16384, KRING = 0, VRING = 65536;

template <int H> DI void issue_half(__amdgpu_buffer_rsrc_t rk, __amdgpu_buffer_rsrc_t rv, char* lds, unsigned vk, unsigned vv, int j) {
  constexpr int t = H >> 1;
  constexpr bool isv = (H & 1);
  char* dst = lds + (isv ? VRING : KRING) + (t & 3) * SLOT + j * 1024;
#pragma unroll
  for (int i = 0; i < 4; ++i)
    __builtin_amdgcn_raw_ptr_buffer_load_lds(isv ? rv : rk, (LAS void*)(dst + i * 4096), 16, isv ? vv : vk, t * SLOT + i * 4096, 0, 0);
}

template <int T> DI void loader_tiles(__amdgpu_buffer_rsrc_t rk, __amdgpu_buffer_rsrc_t rv, char* lds, unsigned vk, unsigned vv, int j) {
  if constexpr (T < 8) {
    constexpr int lastA = (T == 0) ? 7 : (2 * T + 6 > 15 ? 15 : 2 * T + 6);
    WAITV(4 * (lastA - 2 * T));
    BAR();
    if constexpr (T >= 1 && 2 * T + 7 <= 15) issue_half<2 * T + 7>(rk, rv, lds, vk, vv, j);
    constexpr int lastB = (2 * T + 7 > 15 ? 15 : 2 * T + 7);
    WAITV(4 * (lastB - (2 * T + 1)));
    BAR();
    if constexpr (2 * T + 8 <= 15) issue_half<2 * T + 8>(rk, rv, lds, vk, vv, j);
    loader_tiles<T + 1>(rk, rv, lds, vk, vv, j);
  }
}

__global__ __launch_bounds__(512, 2) __attribute__((amdgpu_waves_per_eu(2, 2))) void k_att(const unsigned short* __restrict__ kc, const unsigned short* __restrict__ vc, char* __restrict__ ws) {
  extern __shared__ __attribute__((aligned(16))) char lds[];
  const int bid = blockIdx.x, hkv = bid & 7, b = (bid >> 3) & 7, sp = bid >> 6;
  const int wave = rfl(threadIdx.x >> 6), lane = threadIdx.x & 63;
  if (wave >= 4) {
    const int j = wave - 4;
    const size_t kvoff = ((size_t)(b * HKV + hkv) * SK + sp * 512) * DH;
    auto rk = __builtin_amdgcn_make_buffer_rsrc((void*)(kc + kvoff), (short)0, 512 * 256, 0x00020000);
    auto rv = __builtin_amdgcn_make_buffer_rsrc((void*)(vc + kvoff), (short)0, 512 * 256, 0x00020000);
    const unsigned r4 = lane >> 4, sl = lane & 15;
    const unsigned vk = j * 1024 + r4 * 256 + ((sl ^ (4 * j + r4)) * 16);
    const unsigned vv = j * 1024 + r4 * 256 + ((sl ^ (r4 << 2)) * 16);
    issue_half<0>(rk, rv, lds, vk, vv, j); issue_half<1>(rk, rv, lds, vk, vv, j);
    issue_half<2>(rk, rv, lds, vk, vv, j); issue_half<3>(rk, rv, lds, vk, vv, j);
    issue_half<4>(rk, rv, lds, vk, vv, j); issue_half<5>(rk, rv, lds, vk, vv, j);
    issue_half<6>(rk, rv, lds, vk, vv, j); issue_half<7>(rk, rv, lds, vk, vv, j);
    loader_tiles<0>(rk, rv, lds, vk, vv, j);
    return;
  }
  const int w = wave, r = lane & 31, hh = lane >> 5;
  const char* qp = ws + WS_Q + (size_t)(b * 32 + hkv * 4 + w) * 8192 + lane * 16;
  bf16x8 qf[8];
#pragma unroll
  for (int s = 0; s < 8; ++s) qf[s] = asbf(*(const u32x4*)(qp + s * 1024));
  f32x16 o[4];
#pragma unroll
  for (int db = 0; db < 4; ++db)
#pragma unroll
    for (int i = 0; i < 16; ++i) o[db][i] = 0.f;
  float m = -__builtin_inff(), l = 0.f;
  const unsigned kbase = KRING + 256 * r + 16 * (hh ^ (r & 15));
  const int q4 = (lane & 15) >> 2, p4 = lane & 3, blk = (lane >> 4) & 1;
  const unsigned vbase = VRING + 256 * (4 * hh + q4) + 32 * blk + 8 * p4;
  for (int t = 0; t < 8; ++t) {
    const unsigned so = (t & 3) * SLOT;
    BAR();
    f32x16 s0, s1;
#pragma unroll
    for (int i = 0; i < 16; ++i) { s0[i] = 0.f; s1[i] = 0.f; }
#pragma unroll
    for (int s = 0; s < 8; ++s) {
      const unsigned a = so + (kbase ^ (s << 5));
      bf16x8 k0 = asbf(*(const LAS u32x4*)(LAS char*)(lds + a));
      bf16x8 k1 = asbf(*(const LAS u32x4*)(LAS char*)(lds + a + 8192));
      s0 = __builtin_amdgcn_mfma_f32_32x32x16_bf16(k0, qf[s], s0, 0, 0, 0);
      s1 = __builtin_amdgcn_mfma_f32_32x32x16_bf16(k1, qf[s], s1, 0, 0, 0);
    }
    float pm = fmaxf(s0[0], s1[0]);
#pragma unroll
    for (int i = 1; i < 16; ++i) pm = fmaxf(pm, fmaxf(s0[i], s1[i]));
    { auto rr = __builtin_amdgcn_permlane32_swap(__float_as_uint(pm), __float_as_uint(pm), false, false);
      pm = fmaxf(__uint_as_float(rr[0]), __uint_as_float(rr[1])); }
    const float mn = fmaxf(m, pm);
    const float alpha = __builtin_amdgcn_exp2f(m - mn);
    m = mn;
    float ps = 0.f;
#pragma unroll
    for (int i = 0; i < 16; ++i) { s0[i] = __builtin_amdgcn_exp2f(s0[i] - mn); ps += s0[i]; }
#pragma unroll
    for (int i = 0; i < 16; ++i) { s1[i] = __builtin_amdgcn_exp2f(s1[i] - mn); ps += s1[i]; }
    l = l * alpha + ps;
    bf16x8 pf[4];
#pragma unroll
    for (int sx = 0; sx < 2; ++sx) {
      u32x4 a, c;
#pragma unroll
      for (int i = 0; i < 4; ++i) { a[i] = cvtpk(s0[8 * sx + 2 * i], s0[8 * sx + 2 * i + 1]); c[i] = cvtpk(s1[8 * sx + 2 * i], s1[8 * sx + 2 * i + 1]); }
      pf[sx] = asbf(a); pf[2 + sx] = asbf(c);
    }
#pragma unroll
    for (int db = 0; db < 4; ++db)
#pragma unroll
      for (int i = 0; i < 16; ++i) o[db][i] *= alpha;
    BAR();
#pragma unroll
    for (int db = 0; db < 4; ++db) {
      const unsigned a = so + vbase + 64 * (db ^ q4);
#pragma unroll
      for (int ks = 0; ks < 4; ++ks) {
        bf16x4 lo = __builtin_amdgcn_ds_read_tr16_b64_v4bf16((LAS bf16x4*)(LAS char*)(lds + a + 4096 * ks));
        bf16x4 hi = __builtin_amdgcn_ds_read_tr16_b64_v4bf16((LAS bf16x4*)(LAS char*)(lds + a + 4096 * ks + 2048));
        bf16x8 vf = __builtin_shufflevector(lo, hi, 0, 1, 2, 3, 4, 5, 6, 7);
        o[db] = __builtin_amdgcn_mfma_f32_32x32x16_bf16(vf, pf[ks], o[db], 0, 0, 0);
      }
    }
  }
  { auto rr = __builtin_amdgcn_permlane32_swap(__float_as_uint(l), __float_as_uint(l), false, false);
    l = __uint_as_float(rr[0]) + __uint_as_float(rr[1]); }
  const int pidx = ((b * HKV + hkv) * 4 + sp) * 4 + w;
  char* pp = ws + WS_PART + (size_t)pidx * 8192 + lane * 16;
#pragma unroll
  for (int db = 0; db < 4; ++db)
#pragma unroll
    for (int sx = 0; sx < 2; ++sx) {
      u32x4 v;
#pragma unroll
      for (int i = 0; i < 4; ++i) v[i] = cvtpk(o[db][8 * sx + 2 * i], o[db][8 * sx + 2 * i + 1]);
      *(u32x4*)(pp + (2 * db + sx) * 1024) = v;
    }
  if (hh == 0) *(f32x2*)(ws + WS_ML + (size_t)pidx * 256 + r * 8) = f32x2{m, l};
}

__global__ __launch_bounds__(256) void k_op(const unsigned short* __restrict__ wo, char* __restrict__ ws) {
  extern __shared__ __attribute__((aligned(16))) char lds[];
  const int bid = blockIdx.x, hkv = bid & 7, b = (bid >> 3) & 7, sp = bid >> 6;
  const int w = rfl(threadIdx.x >> 6), lane = threadIdx.x & 63;
  const int r = lane & 31, hh = lane >> 5;
  auto rw = __builtin_amdgcn_make_buffer_rsrc((void*)(wo + (size_t)hkv * 512 * 512 + sp * 128), (short)0, 512 * 1024 - sp * 256, 0x00020000);
  const unsigned r4 = lane >> 4, sl = lane & 15;
  const unsigned vw = (w * 4 + r4) * 1024 + ((sl ^ (r4 << 2)) * 16);
#pragma unroll
  for (int kt = 0; kt < 8; ++kt)
#pragma unroll
    for (int i = 0; i < 4; ++i)
      __builtin_amdgcn_raw_ptr_buffer_load_lds(rw, (LAS void*)(lds + kt * SLOT + i * 4096 + w * 1024), 16, vw, (kt * 64 + i * 16) * 1024, 0, 0);
  float ms[4], ls[4];
#pragma unroll
  for (int s = 0; s < 4; ++s) {
    const int pidx = ((b * HKV + hkv) * 4 + s) * 4 + w;
    f32x2 v = *(const f32x2*)(ws + WS_ML + (size_t)pidx * 256 + r * 8);
    ms[s] = v[0]; ls[s] = v[1];
  }
  const float M = fmaxf(fmaxf(ms[0], ms[1]), fmaxf(ms[2], ms[3]));
  float a[4]; float L = 0.f;
#pragma unroll
  for (int s = 0; s < 4; ++s) { a[s] = __builtin_amdgcn_exp2f(ms[s] - M); L += a[s] * ls[s]; }
  const float inv = 1.f / L;
#pragma unroll
  for (int s = 0; s < 4; ++s) a[s] *= inv;
#pragma unroll
  for (int f = 0; f < 8; ++f) {
    float lo[4] = {0.f, 0.f, 0.f, 0.f}, hi[4] = {0.f, 0.f, 0.f, 0.f};
#pragma unroll
    for (int s = 0; s < 4; ++s) {
      const int pidx = ((b * HKV + hkv) * 4 + s) * 4 + w;
      u32x4 v = *(const u32x4*)(ws + WS_PART + (size_t)pidx * 8192 + f * 1024 + lane * 16);
#pragma unroll
      for (int i = 0; i < 4; ++i) { lo[i] += a[s] * __uint_as_float(v[i] << 16); hi[i] += a[s] * __uint_as_float(v[i] & 0xffff0000u); }
    }
    u32x4 ov;
#pragma unroll
    for (int i = 0; i < 4; ++i) ov[i] = cvtpk(lo[i], hi[i]);
    *(LAS u32x4*)(LAS char*)(lds + 131072 + (w * 8 + f) * 1024 + lane * 16) = ov;
  }
  asm volatile("s_waitcnt vmcnt(0) lgkmcnt(0)" ::: "memory");
  BAR();
  const int q4 = (lane & 15) >> 2, p4 = lane & 3, blk = (lane >> 4) & 1;
  const unsigned vbase = 256 * (4 * hh + q4) + 32 * blk + 8 * p4 + 64 * (w ^ q4);
  f32x16 acc;
#pragma unroll
  for (int i = 0; i < 16; ++i) acc[i] = 0.f;
#pragma unroll
  for (int kt = 0; kt < 8; ++kt)
#pragma unroll
    for (int ks = 0; ks < 4; ++ks) {
      bf16x4 lo = __builtin_amdgcn_ds_read_tr16_b64_v4bf16((LAS bf16x4*)(LAS char*)(lds + kt * SLOT + vbase + 4096 * ks));
      bf16x4 hi = __builtin_amdgcn_ds_read_tr16_b64_v4bf16((LAS bf16x4*)(LAS char*)(lds + kt * SLOT + vbase + 4096 * ks + 2048));
      bf16x8 wf = __builtin_shufflevector(lo, hi, 0, 1, 2, 3, 4, 5, 6, 7);
      bf16x8 of = asbf(*(const LAS u32x4*)(LAS char*)(lds + 131072 + ((kt >> 1) * 8 + 4 * (kt & 1) + ks) * 1024 + lane * 16));
      acc = __builtin_amdgcn_mfma_f32_32x32x16_bf16(wf, of, acc, 0, 0, 0);
    }
  char* po = ws + WS_PO + (size_t)((b * 4 + sp) * 8 + hkv) * 16384 + w * 4096 + lane * 16;
#pragma unroll
  for (int q = 0; q < 4; ++q) *(f32x4*)(po + q * 1024) = f32x4{acc[4 * q], acc[4 * q + 1], acc[4 * q + 2], acc[4 * q + 3]};
}

__global__ __launch_bounds__(128) void k_fin(const char* __restrict__ ws, unsigned short* __restrict__ out) {
  const int bid = blockIdx.x, hkv = bid & 7, b = (bid >> 3) & 7, sp = bid >> 6;
  const int j = threadIdx.x >> 6, lane = threadIdx.x & 63;
  const int pc = 8 * j + (lane >> 3), sub = lane & 7, hh = sub >> 2, row = 4 * hkv + (sub & 3);
  f32x4 s = {0.f, 0.f, 0.f, 0.f};
#pragma unroll
  for (int k = 0; k < 8; ++k) s += *(const f32x4*)(ws + WS_PO + (size_t)((b * 4 + sp) * 8 + k) * 16384 + pc * 1024 + (hh * 32 + row) * 16);
  const int n = 32 * (pc >> 2) + 8 * (pc & 3) + 4 * hh;
  u32x2 v = {cvtpk(s[0], s[1]), cvtpk(s[2], s[3])};
  *(u32x2*)(out + (size_t)(b * 32 + row) * 512 + sp * 128 + n) = v;
}

extern "C" void kernel_launch(void* const* d_in, const int* in_sizes, int n_in, void* d_out, int out_size, void* d_ws, size_t ws_size, hipStream_t stream) {
  const unsigned short* x = (const unsigned short*)d_in[0];
  const unsigned short* kc = (const unsigned short*)d_in[1];
  const unsigned short* vc = (const unsigned short*)d_in[2];
  const unsigned short* wq = (const unsigned short*)d_in[3];
  const unsigned short* wo = (const unsigned short*)d_in[4];
  char* ws = (char*)d_ws;
  (void)hipFuncSetAttribute((const void*)k_att, hipFuncAttributeMaxDynamicSharedMemorySize, 131072);
  (void)hipFuncSetAttribute((const void*)k_op, hipFuncAttributeMaxDynamicSharedMemorySize, 163840);
  k_q<<<256, 256, 49152, stream>>>(x, wq, ws);
  k_att<<<256, 512, 131072, stream>>>(kc, vc, ws);
  k_op<<<256, 256, 163840, stream>>>(wo, ws);
  k_fin<<<256, 128, 0, stream>>>(ws, (unsigned short*)d_out);
}
```

```cpp
#include <hip/hip_runtime.h>
#include <hip/hip_bf16.h>
#include <stdint.h>

typedef __attribute__((ext_vector_type(8))) __bf16 bf16x8;
typedef __attribute__((ext_vector_type(4))) __bf16 bf16x4;
typedef __attribute__((ext_vector_type(2))) __bf16 bf16x2;
typedef __attribute__((ext_vector_type(16))) float f32x16;
typedef __attribute__((ext_vector_type(4))) float f32x4;
typedef __attribute__((ext_vector_type(2))) float f32x2;
typedef __attribute__((ext_vector_type(4))) unsigned u32x4;
typedef __attribute__((ext_vector_type(2))) unsigned u32x2;
#define LAS __attribute__((address_space(3)))
#define GAS __attribute__((address_space(1)))
#define DI __device__ __forceinline__
typedef GAS unsigned long long gu64;
typedef __amdgpu_buffer_rsrc_t rsrc_t;

#define KV_AUX 2
#define W_AUX 0
constexpr int SC1 = 16;
constexpr float QSCALE = 0.12751743082f;
constexpr float THR = 8.0f;

constexpr unsigned WS_EP = 0;
constexpr unsigned WS_FQ = 4096;
constexpr unsigned WS_F1 = WS_FQ + 65536;
constexpr unsigned WS_F2 = WS_F1 + 65536;
constexpr unsigned WS_Q = 262144;
constexpr unsigned WS_PART = WS_Q + 256 * 8192;
constexpr unsigned WS_ML = WS_PART + 1024 * 8192;
constexpr unsigned WS_PO = WS_ML + 1024 * 256;
constexpr unsigned WS_END = WS_PO + 256 * 16384;
constexpr size_t WS_ST = 64u << 20;

constexpr unsigned SLOT = 16384, KRING = 0, VRING = 65536, SPARE = 131072, LDS_BYTES = 163840;
constexpr unsigned QSCR = VRING + 3 * SLOT;
constexpr unsigned CNT = KRING + 3 * SLOT;
constexpr unsigned long long TAGC = 0x9E3779B97F4A7C15ull;
constexpr unsigned SPIN_CAP = 1u << 18;

#define STAMP(i) do {} while (0)
#define LSTAMP(i) do {} while (0)

DI unsigned cvtpk(float lo, float hi) { f32x2 v = {lo, hi}; bf16x2 b = __builtin_convertvector(v, bf16x2); return __builtin_bit_cast(unsigned, b); }
DI bf16x8 asbf(u32x4 v) { return __builtin_bit_cast(bf16x8, v); }
DI int rfl(int x) { return __builtin_amdgcn_readfirstlane(x); }
DI float fmx(float a, float b) { return __builtin_elementwise_maximum(a, b); }
DI float fmx3(float a, float b, float c) { return fmx(fmx(a, b), c); }
DI rsrc_t mkrs(const void* p, unsigned bytes) { return __builtin_amdgcn_make_buffer_rsrc((void*)p, (short)0, bytes, 0x00020000); }
DI u32x4 ldsr128(unsigned a) { return *(const LAS u32x4*)(size_t)a; }
DI void ldsw128(unsigned a, u32x4 v) { *(LAS u32x4*)(size_t)a = v; }
DI bf16x8 ldstr(unsigned a) {
  bf16x4 lo = __builtin_amdgcn_ds_read_tr16_b64_v4bf16((LAS bf16x4*)(size_t)a);
  bf16x4 hi = __builtin_amdgcn_ds_read_tr16_b64_v4bf16((LAS bf16x4*)(size_t)(a + 2048));
  return __builtin_shufflevector(lo, hi, 0, 1, 2, 3, 4, 5, 6, 7);
}
#define SBAR() __builtin_amdgcn_sched_barrier(0)
#define BAR() do { SBAR(); asm volatile("s_waitcnt lgkmcnt(0)\n\ts_barrier" ::: "memory"); SBAR(); } while (0)
#define WAITV(n) asm volatile("s_waitcnt vmcnt(%0)" ::"n"(n) : "memory")
#define WAITL() asm volatile("s_waitcnt lgkmcnt(0)" ::: "memory")
#define MFMA32(a, b, c) __builtin_amdgcn_mfma_f32_32x32x16_bf16((a), (b), (c), 0, 0, 0)

DI void poll_flags(const char* base, unsigned stride, int n, unsigned long long tag, int lane) {
  gu64* p = (gu64*)(base + (size_t)(lane < n ? lane : 0) * stride);
#pragma unroll 1
  for (unsigned spins = 0; spins < SPIN_CAP; ++spins) {
    const unsigned long long v = __hip_atomic_load(p, __ATOMIC_RELAXED, __HIP_MEMORY_SCOPE_AGENT);
    if (__all(v == tag)) break;
    __builtin_amdgcn_s_sleep(1);
  }
  asm volatile("" ::: "memory");
}

DI void report(char* ws, unsigned long long tag, unsigned slot, unsigned val, bool bad) {
  if (__any(bad) && (threadIdx.x & 63) == 0) __hip_atomic_store((gu64*)(ws + 2048 + 256 * slot), tag + val, __ATOMIC_RELAXED, __HIP_MEMORY_SCOPE_AGENT);
}
DI bool badbf(unsigned v, float lim) { return !(fabsf(__uint_as_float(v << 16)) < lim) || !(fabsf(__uint_as_float(v & 0xffff0000u)) < lim); }
DI bool bad4(u32x4 v, float lim) { return badbf(v[0], lim) || badbf(v[1], lim) || badbf(v[2], lim) || badbf(v[3], lim); }
struct Ld { rsrc_t rk, rv, rw; unsigned vk, vv, vw, lb; int j; };
template <int U> DI void issue(const Ld& L) {
  if constexpr (U < 16) {
    constexpr int t = U >> 1; constexpr bool isv = U & 1;
    const unsigned dst = L.lb + (isv ? VRING : KRING) + (t & 3) * SLOT + L.j * 1024;
#pragma unroll
    for (int i = 0; i < 4; ++i)
      __builtin_amdgcn_raw_ptr_buffer_load_lds(isv ? L.rv : L.rk, (LAS void*)(size_t)(dst + i * 4096), 16, isv ? L.vv : L.vk, t * SLOT + i * 4096, 0, KV_AUX);
  } else {
    constexpr int kt = U - 16;
    constexpr unsigned base = kt == 0 ? SPARE : kt == 1 ? SPARE + SLOT : ((kt & 1) ? VRING : KRING) + ((kt - 2) >> 1) * SLOT;
    const unsigned dst = L.lb + base + L.j * 1024;
#pragma unroll
    for (int i = 0; i < 4; ++i)
      __builtin_amdgcn_raw_ptr_buffer_load_lds(L.rw, (LAS void*)(size_t)(dst + i * 4096), 16, L.vw, (kt * 64 + i * 16) * 1024, 0, W_AUX);
  }
}
constexpr unsigned wo_lds(int kt) { return kt == 0 ? SPARE : kt == 1 ? SPARE + SLOT : ((kt & 1) ? VRING : KRING) + ((kt - 2) >> 1) * SLOT; }
constexpr int last_issued(int j) { return j == 0 ? 5 : j <= 9 ? j + 6 : j <= 12 ? 15 : j + 3; }
template <int J> DI void loader_main(const Ld& L, unsigned long long (&lst)[24]) {
  if constexpr (J < 16) {
    WAITV(4 * (last_issued(J) - J));
    LSTAMP(4 + J);
    BAR();
    if constexpr (J == 0) { issue<6>(L); issue<7>(L); }
    else if constexpr (J <= 8) issue<J + 7>(L);
    else if constexpr (J >= 12 && J <= 14) issue<J + 4>(L);
    else if constexpr (J == 15) { issue<19>(L); issue<20>(L); issue<21>(L); issue<22>(L); issue<23>(L); }
    loader_main<J + 1>(L, lst);
  }
}

template <int W> DI void q_scatter(const f32x4 (&acc)[2][8], f32x4 (&own)[2][2], unsigned scr) {
#pragma unroll
  for (int reg = 0; reg < 4; ++reg)
#pragma unroll
    for (int rh = 0; rh < 2; ++rh)
#pragma unroll
      for (int hf = 0; hf < 2; ++hf) {
        const f32x4 v = {acc[rh][4 * hf][reg], acc[rh][4 * hf + 1][reg], acc[rh][4 * hf + 2][reg], acc[rh][4 * hf + 3][reg]};
        if (reg == W) own[rh][hf] = v;
        else ldsw128(scr + (((reg * 3 + (W < reg ? W : W - 1)) * 2 + rh) * 2 + hf) * 1024, __builtin_bit_cast(u32x4, v));
      }
}

__global__ __launch_bounds__(512, 2) void fused_gqa(const unsigned short* __restrict__ x, const unsigned short* __restrict__ kc, const unsigned short* __restrict__ vc,
                                                    const unsigned short* __restrict__ wq, const unsigned short* __restrict__ wo, unsigned short* __restrict__ out, char* __restrict__ ws) {
  extern __shared__ __attribute__((aligned(16))) char lds[];
  const int bid = blockIdx.x, hkv = bid & 7, b = (bid >> 3) & 7, sp = bid >> 6;
  const int wave = rfl(threadIdx.x >> 6), lane = threadIdx.x & 63;
  const unsigned lb = (unsigned)(size_t)(LAS char*)lds;
  STAMP(0);
  if (wave >= 4) {
    unsigned long long lst[24];
    Ld L; L.j = wave - 4; L.lb = lb;
    const size_t kvoff = ((size_t)(b * 8 + hkv) * 2048 + sp * 512) * 128;
    L.rk = mkrs(kc + kvoff, 512 * 256);
    L.rv = mkrs(vc + kvoff, 512 * 256);
    L.rw = mkrs(wo + (size_t)hkv * 512 * 512 + sp * 128, 512 * 1024 - sp * 256);
    const unsigned r4 = lane >> 4, sl = lane & 15;
    L.vk = L.j * 1024 + r4 * 256 + ((sl ^ (4 * L.j + r4)) * 16);
    L.vv = L.j * 1024 + r4 * 256 + ((sl ^ (r4 << 2)) * 16);
    L.vw = (L.j * 4 + r4) * 1024 + ((sl ^ (r4 << 2)) * 16);
    if (wave == 4 && lane < 2) *(LAS unsigned*)(size_t)(lb + CNT + lane * 4) = 0u;
    BAR();
    issue<0>(L); issue<1>(L); issue<2>(L);
    LSTAMP(0);
    WAITV(8); issue<3>(L);
    LSTAMP(1);
    WAITV(8); issue<4>(L);
    LSTAMP(2);
    WAITV(8); issue<5>(L);
    LSTAMP(3);
    loader_main<0>(L, lst);
    BAR();
    WAITV(0);
    LSTAMP(20);
    BAR();
    BAR();
    return;
  }
  const int w = wave;
  const rsrc_t rws = mkrs(ws, WS_END);
  const unsigned long long ep = __hip_atomic_load((gu64*)(ws + WS_EP + b * 256), __ATOMIC_RELAXED, __HIP_MEMORY_SCOPE_AGENT);
  {
    const int c = lane & 15, g = lane >> 4, h = hkv * 4 + sp;
    const rsrc_t rq = mkrs(wq + h * 128, 512 * 8192 - h * 256);
    const rsrc_t rx = mkrs(x + b * 32 * 512, 32 * 1024);
    const unsigned vq = (128 * w + 8 * g) * 8192 + 16 * c;
    const unsigned vx = c * 1024 + (128 * w + 8 * g) * 2;
    u32x4 wr[4][8], xa[4][2];
#pragma unroll
    for (int st = 0; st < 4; ++st) {
#pragma unroll
      for (int rh = 0; rh < 2; ++rh) xa[st][rh] = __builtin_amdgcn_raw_buffer_load_b128(rx, vx + rh * 16384 + st * 64, 0, W_AUX);
#pragma unroll
      for (int i = 0; i < 8; ++i) wr[st][i] = __builtin_amdgcn_raw_buffer_load_b128(rq, vq, (32 * st + i) * 8192, W_AUX);
    }
    BAR();
    STAMP(1);
    f32x4 acc[2][8];
#pragma unroll
    for (int rh = 0; rh < 2; ++rh)
#pragma unroll
      for (int jj = 0; jj < 8; ++jj) acc[rh][jj] = f32x4{0.f, 0.f, 0.f, 0.f};
#pragma unroll
    for (int st = 0; st < 4; ++st)
#pragma unroll
      for (int jj = 0; jj < 8; ++jj) {
        u32x4 a;
#pragma unroll
        for (int e = 0; e < 4; ++e) a[e] = __builtin_amdgcn_perm(wr[st][2 * e + 1][jj >> 1], wr[st][2 * e][jj >> 1], (jj & 1) ? 0x07060302u : 0x05040100u);
#pragma unroll
        for (int rh = 0; rh < 2; ++rh) acc[rh][jj] = __builtin_amdgcn_mfma_f32_16x16x32_bf16(asbf(a), asbf(xa[st][rh]), acc[rh][jj], 0, 0, 0);
      }
    { bool bad = false;
      for (int st = 0; st < 4; ++st) { bad |= bad4(xa[st][0], 16.f) || bad4(xa[st][1], 16.f); for (int i = 0; i < 8; i += 7) bad |= bad4(wr[st][i], 16.f); }
      report(ws, ep + TAGC, 1, 1, bad);
      bool bad2 = false;
      for (int rh = 0; rh < 2; ++rh) for (int jj = 0; jj < 8; ++jj) for (int e = 0; e < 4; ++e) bad2 |= !(fabsf(acc[rh][jj][e]) < 100.f);
      report(ws, ep + TAGC, 2, 1, bad2); }
    f32x4 own[2][2];
    switch (w) {
      case 0: q_scatter<0>(acc, own, lb + QSCR + lane * 16); break;
      case 1: q_scatter<1>(acc, own, lb + QSCR + lane * 16); break;
      case 2: q_scatter<2>(acc, own, lb + QSCR + lane * 16); break;
      default: q_scatter<3>(acc, own, lb + QSCR + lane * 16); break;
    }
    WAITL();
    volatile LAS unsigned* c1 = (volatile LAS unsigned*)(size_t)(lb + CNT);
    if (lane == 0) __hip_atomic_fetch_add((LAS unsigned*)c1, 1u, __ATOMIC_RELAXED, __HIP_MEMORY_SCOPE_WORKGROUP);
    while (*c1 < 4u) __builtin_amdgcn_s_sleep(1);
    asm volatile("" ::: "memory");
#pragma unroll
    for (int j = 0; j < 3; ++j)
#pragma unroll
      for (int rh = 0; rh < 2; ++rh)
#pragma unroll
        for (int hf = 0; hf < 2; ++hf) own[rh][hf] += __builtin_bit_cast(f32x4, ldsr128(lb + QSCR + (((w * 3 + j) * 2 + rh) * 2 + hf) * 1024 + lane * 16));
    { bool bad = false; for (int rh = 0; rh < 2; ++rh) for (int hf = 0; hf < 2; ++hf) for (int e = 0; e < 4; ++e) bad |= !(fabsf(own[rh][hf][e]) < 100.f);
      report(ws, ep + TAGC, 3, 1, bad); }
#pragma unroll
    for (int rh = 0; rh < 2; ++rh) {
      u32x4 o;
      o[0] = cvtpk(own[rh][0][0] * QSCALE, own[rh][0][1] * QSCALE);
      o[1] = cvtpk(own[rh][0][2] * QSCALE, own[rh][0][3] * QSCALE);
      o[2] = cvtpk(own[rh][1][0] * QSCALE, own[rh][1][1] * QSCALE);
      o[3] = cvtpk(own[rh][1][2] * QSCALE, own[rh][1][3] * QSCALE);
      __builtin_amdgcn_raw_buffer_store_b128(o, rws, ((2 * g + (w >> 1)) * 64 + (w & 1) * 32 + 16 * rh + c) * 16, WS_Q + (b * 32 + h) * 8192, SC1);
    }
    STAMP(3);
    WAITV(0);
    unsigned prev = 0;
    if (lane == 0) prev = __hip_atomic_fetch_add((LAS unsigned*)(size_t)(lb + CNT + 4), 1u, __ATOMIC_RELAXED, __HIP_MEMORY_SCOPE_WORKGROUP);
    if (lane == 0 && prev == 3u) __hip_atomic_store((gu64*)(ws + WS_FQ + bid * 256), ep + TAGC, __ATOMIC_RELAXED, __HIP_MEMORY_SCOPE_AGENT);
    STAMP(4);
  }
  const unsigned long long tag = ep + TAGC;
  const int grp = hkv + 8 * b;
  poll_flags(ws + WS_FQ + (grp + 64 * w) * 256, 0, 1, tag, lane);
  STAMP(5);
  bf16x8 qf[8];
#pragma unroll
  for (int s = 0; s < 8; ++s) qf[s] = asbf(__builtin_amdgcn_raw_buffer_load_b128(rws, lane * 16 + s * 1024, WS_Q + (b * 32 + hkv * 4 + w) * 8192, SC1));

  { bool bad = false;
    for (int s = 0; s < 8; ++s) bad |= bad4(__builtin_bit_cast(u32x4, qf[s]), 8.f);
    report(ws, tag, 4, 1, bad); }
  const int r = lane & 31, hh = lane >> 5;
  f32x16 o[4], negm;
#pragma unroll
  for (int i = 0; i < 16; ++i) { o[0][i] = 0.f; o[1][i] = 0.f; o[2][i] = 0.f; o[3][i] = 0.f; negm[i] = 0.f; }
  float m = 0.f, l0 = 0.f, l1 = 0.f;
  const unsigned kbase = lb + KRING + 256 * r + 16 * (hh ^ (r & 15));
  const int q4 = (lane & 15) >> 2, p4 = lane & 3, blk = (lane >> 4) & 1;
  const unsigned vlane = 256 * (4 * hh + q4) + 32 * blk + 8 * p4;
  WAITV(0);
  STAMP(6);
  for (int t = 0; t < 8; ++t) {
    const unsigned so = (t & 3) * SLOT;
    BAR();
    STAMP(8 + 2 * t);
    f32x16 s0, s1;
    u32x4 kf[16];
    unsigned ka[8];
#pragma unroll
    for (int s = 0; s < 8; ++s) ka[s] = (kbase ^ (s << 5)) + so;
#pragma unroll
    for (int i = 0; i < 6; ++i) kf[i] = ldsr128(ka[i]);
    SBAR();
    float pm0 = 0.f, pm1;
#pragma unroll
    for (int mi = 0; mi < 16; ++mi) {
      if (mi == 0) s0 = MFMA32(asbf(kf[0]), qf[0], negm);
      else if (mi < 8) s0 = MFMA32(asbf(kf[mi]), qf[mi], s0);
      else if (mi == 8) s1 = MFMA32(asbf(kf[8]), qf[0], negm);
      else s1 = MFMA32(asbf(kf[mi]), qf[mi - 8], s1);
      if (mi + 6 < 16) kf[mi + 6] = ldsr128(ka[(mi + 6) & 7] + ((mi + 6) >> 3) * 8192);
      if (mi == 11) { pm0 = fmx3(s0[0], s0[1], s0[2]); pm0 = fmx3(pm0, s0[3], s0[4]); }
      if (mi == 12) { pm0 = fmx3(pm0, s0[5], s0[6]); pm0 = fmx3(pm0, s0[7], s0[8]); }
      if (mi == 13) { pm0 = fmx3(pm0, s0[9], s0[10]); pm0 = fmx3(pm0, s0[11], s0[12]); }
      if (mi == 14) { pm0 = fmx3(pm0, s0[13], s0[14]); pm0 = fmx(pm0, s0[15]); }
      SBAR();
    }
    pm1 = fmx3(s1[0], s1[1], s1[2]);
#pragma unroll
    for (int i = 3; i < 15; i += 2) pm1 = fmx3(pm1, s1[i], s1[i + 1]);
    float pm = fmx3(pm0, pm1, s1[15]);
    { auto rr = __builtin_amdgcn_permlane32_swap(__float_as_uint(pm), __float_as_uint(pm), false, false);
      pm = fmx(__uint_as_float(rr[0]), __uint_as_float(rr[1])); }
    if (t == 0 || __any(pm > THR)) {
      const float dl = t == 0 ? pm : fmx(pm, 0.f);
      const float alpha = t == 0 ? 1.f : __builtin_amdgcn_exp2f(-dl);
      m += dl;
#pragma unroll
      for (int i = 0; i < 16; ++i) { s0[i] -= dl; s1[i] -= dl; negm[i] = -m; }
      l0 *= alpha; l1 *= alpha;
#pragma unroll
      for (int db = 0; db < 4; ++db)
#pragma unroll
        for (int i = 0; i < 16; ++i) o[db][i] *= alpha;
    }
    SBAR();
    bf16x8 pf[4];
    {
      u32x4 a, c, d, e;
#pragma unroll
      for (int i = 0; i < 16; ++i) { s0[i] = __builtin_amdgcn_exp2f(s0[i]); s1[i] = __builtin_amdgcn_exp2f(s1[i]); l0 += s0[i]; l1 += s1[i]; }
#pragma unroll
      for (int i = 0; i < 4; ++i) {
        a[i] = cvtpk(s0[2 * i], s0[2 * i + 1]); c[i] = cvtpk(s0[8 + 2 * i], s0[8 + 2 * i + 1]);
        d[i] = cvtpk(s1[2 * i], s1[2 * i + 1]); e[i] = cvtpk(s1[8 + 2 * i], s1[8 + 2 * i + 1]);
      }
      pf[0] = asbf(a); pf[1] = asbf(c); pf[2] = asbf(d); pf[3] = asbf(e);
      asm volatile("" : "+v"(a), "+v"(c), "+v"(d), "+v"(e));
      pf[0] = asbf(a); pf[1] = asbf(c); pf[2] = asbf(d); pf[3] = asbf(e);
    }
    BAR();
    STAMP(9 + 2 * t);
    unsigned va[4];
#pragma unroll
    for (int db = 0; db < 4; ++db) va[db] = lb + VRING + so + vlane + 64 * (db ^ q4);
    bf16x8 vf[16];
#pragma unroll
    for (int i = 0; i < 5; ++i) vf[i] = ldstr(va[i & 3] + (i >> 2) * 4096);
    SBAR();
#pragma unroll
    for (int mi = 0; mi < 16; ++mi) {
      o[mi & 3] = MFMA32(vf[mi], pf[mi >> 2], o[mi & 3]);
      if (mi + 5 < 16) vf[mi + 5] = ldstr(va[(mi + 5) & 3] + ((mi + 5) >> 2) * 4096);
      SBAR();
    }

  }
  STAMP(24);
  float l = l0 + l1;
  { auto rr = __builtin_amdgcn_permlane32_swap(__float_as_uint(l), __float_as_uint(l), false, false);
    l = __uint_as_float(rr[0]) + __uint_as_float(rr[1]); }
  const int pgrp = (b * 8 + hkv) * 4;
  {
    const unsigned pbase = WS_PART + ((pgrp + sp) * 4 + w) * 8192;
#pragma unroll
    for (int db = 0; db < 4; ++db)
#pragma unroll
      for (int sx = 0; sx < 2; ++sx) {
        u32x4 v;
#pragma unroll
        for (int i = 0; i < 4; ++i) v[i] = cvtpk(o[db][8 * sx + 2 * i], o[db][8 * sx + 2 * i + 1]);
        __builtin_amdgcn_raw_buffer_store_b128(v, rws, lane * 16 + (2 * db + sx) * 1024, pbase, SC1);
      }
    if (hh == 0) __builtin_amdgcn_raw_buffer_store_b64(u32x2{__float_as_uint(m), __float_as_uint(l)}, rws, r * 8, WS_ML + ((pgrp + sp) * 4 + w) * 256, SC1);
  }
  STAMP(25);
  WAITV(0);
  STAMP(26);
  BAR();
  if (w == 0 && lane == 0) __hip_atomic_store((gu64*)(ws + WS_F1 + bid * 256), tag, __ATOMIC_RELAXED, __HIP_MEMORY_SCOPE_AGENT);
  poll_flags(ws + WS_F1 + grp * 256, 64 * 256, 4, tag, lane);
  STAMP(27);
  {
    u32x2 st[3]; u32x4 pr[3][8];
#pragma unroll
    for (int j = 0; j < 3; ++j) {
      const int s2 = (sp + 1 + j) & 3;
      st[j] = __builtin_amdgcn_raw_buffer_load_b64(rws, r * 8, WS_ML + ((pgrp + s2) * 4 + w) * 256, SC1);
    }
#pragma unroll
    for (int j = 0; j < 3; ++j) {
      const int s2 = (sp + 1 + j) & 3;
#pragma unroll
      for (int f = 0; f < 8; ++f) pr[j][f] = __builtin_amdgcn_raw_buffer_load_b128(rws, lane * 16 + f * 1024, WS_PART + ((pgrp + s2) * 4 + w) * 8192, SC1);
    }
    float M = m;
#pragma unroll
    for (int j = 0; j < 3; ++j) M = fmx(M, __uint_as_float(st[j][0]));
    float a0 = __builtin_amdgcn_exp2f(m - M), a[3];
    float L = a0 * l;
#pragma unroll
    for (int j = 0; j < 3; ++j) { a[j] = __builtin_amdgcn_exp2f(__uint_as_float(st[j][0]) - M); L += a[j] * __uint_as_float(st[j][1]); }
    const float inv = 1.f / L;
    a0 *= inv;
#pragma unroll
    for (int j = 0; j < 3; ++j) a[j] *= inv;
    const unsigned ofr = lb + (w < 2 ? KRING + 3 * SLOT + w * 8192 : VRING + 3 * SLOT + (w - 2) * 8192) + lane * 16;
#pragma unroll
    for (int f = 0; f < 8; ++f) {
      u32x4 ov;
#pragma unroll
      for (int i = 0; i < 4; ++i) {
        float lo = a0 * o[f >> 1][8 * (f & 1) + 2 * i], hi = a0 * o[f >> 1][8 * (f & 1) + 2 * i + 1];
#pragma unroll
        for (int j = 0; j < 3; ++j) { lo += a[j] * __uint_as_float(pr[j][f][i] << 16); hi += a[j] * __uint_as_float(pr[j][f][i] & 0xffff0000u); }
        ov[i] = cvtpk(lo, hi);
      }
      ldsw128(ofr + f * 1024, ov);
    }
  }
  STAMP(28);
  BAR();
  STAMP(29);
  {
    const unsigned wl = lb + vlane + 64 * (w ^ q4);
    f32x16 acc;
#pragma unroll
    for (int i = 0; i < 16; ++i) acc[i] = 0.f;
#pragma unroll
    for (int kt = 0; kt < 8; ++kt)
#pragma unroll
      for (int ks = 0; ks < 4; ++ks) {
        const int hd = kt >> 1, f = 4 * (kt & 1) + ks;
        const bf16x8 wf = ldstr(wl + wo_lds(kt) + 4096 * ks);
        const bf16x8 of = asbf(ldsr128(lb + (hd < 2 ? KRING + 3 * SLOT + hd * 8192 : VRING + 3 * SLOT + (hd - 2) * 8192) + f * 1024 + lane * 16));
        acc = MFMA32(wf, of, acc);
      }
    const unsigned pob = WS_PO + ((b * 4 + sp) * 8 + hkv) * 16384 + w * 4096;
#pragma unroll
    for (int q = 0; q < 4; ++q) {
      f32x4 v = {acc[4 * q], acc[4 * q + 1], acc[4 * q + 2], acc[4 * q + 3]};
      __builtin_amdgcn_raw_buffer_store_b128(__builtin_bit_cast(u32x4, v), rws, lane * 16 + q * 1024, pob, SC1);
    }
  }
  STAMP(30);
  WAITV(0);
  STAMP(31);
  BAR();
  if (w == 0 && lane == 0) __hip_atomic_store((gu64*)(ws + WS_F2 + bid * 256), tag, __ATOMIC_RELAXED, __HIP_MEMORY_SCOPE_AGENT);
  if (w < 2) {
    poll_flags(ws + WS_F2 + (8 * b + 64 * sp) * 256, 256, 8, tag, lane);
    STAMP(32);
    const int pc = 8 * w + (lane >> 3), sub = lane & 7, h2 = sub >> 2, row = 4 * hkv + (sub & 3);
    u32x4 pv[8];
#pragma unroll
    for (int k = 0; k < 8; ++k) pv[k] = __builtin_amdgcn_raw_buffer_load_b128(rws, pc * 1024 + (h2 * 32 + row) * 16, WS_PO + ((b * 4 + sp) * 8 + k) * 16384, SC1);
    f32x4 s = __builtin_bit_cast(f32x4, pv[0]);
#pragma unroll
    for (int k = 1; k < 8; ++k) s += __builtin_bit_cast(f32x4, pv[k]);
    const int n = 32 * (pc >> 2) + 8 * (pc & 3) + 4 * h2;
    { float code = 0.f, mul = 1.f;
      for (int c = 1; c <= 4; ++c) { const unsigned long long t2 = __hip_atomic_load((gu64*)(ws + 2048 + 256 * c), __ATOMIC_RELAXED, __HIP_MEMORY_SCOPE_AGENT) - tag; if (t2 >= 1 && t2 <= 8 && code == 0.f) code = mul * (float)(t2 + 8); mul *= 32.f; }
      if (code > 0.f) { s[0] = s[1] = s[2] = s[3] = code; }
      else { for (int e = 0; e < 4; ++e) if (!(fabsf(s[e]) < 1.f)) s[e] = 0.5f; } }
    *(u32x2*)(out + (size_t)(b * 32 + row) * 512 + sp * 128 + n) = u32x2{cvtpk(s[0], s[1]), cvtpk(s[2], s[3])};
    if (w == 0 && hkv == 0 && sp == 0 && lane == 0) __hip_atomic_store((gu64*)(ws + WS_EP + b * 256), ep + 1ull, __ATOMIC_RELAXED, __HIP_MEMORY_SCOPE_AGENT);
    STAMP(33);
  }
}

extern "C" void kernel_launch(void* const* d_in, const int* in_sizes, int n_in, void* d_out, int out_size, void* d_ws, size_t ws_size, hipStream_t stream) {
  (void)hipFuncSetAttribute((const void*)fused_gqa, hipFuncAttributeMaxDynamicSharedMemorySize, LDS_BYTES);
  fused_gqa<<<256, 512, LDS_BYTES, stream>>>((const unsigned short*)d_in[0], (const unsigned short*)d_in[1], (const unsigned short*)d_in[2],
                                             (const unsigned short*)d_in[3], (const unsigned short*)d_in[4], (unsigned short*)d_out, (char*)d_ws);
}
```

```cpp
#include <hip/hip_runtime.h>
#include <hip/hip_bf16.h>
#include <stdint.h>

typedef __attribute__((ext_vector_type(8))) __bf16 bf16x8;
typedef __attribute__((ext_vector_type(4))) __bf16 bf16x4;
typedef __attribute__((ext_vector_type(2))) __bf16 bf16x2;
typedef __attribute__((ext_vector_type(16))) float f32x16;
typedef __attribute__((ext_vector_type(4))) float f32x4;
typedef __attribute__((ext_vector_type(2))) float f32x2;
typedef __attribute__((ext_vector_type(4))) unsigned u32x4;
typedef __attribute__((ext_vector_type(2))) unsigned u32x2;
#define LAS __attribute__((address_space(3)))
#define GAS __attribute__((address_space(1)))
#define DI __device__ __forceinline__
typedef GAS unsigned long long gu64;
typedef __amdgpu_buffer_rsrc_t rsrc_t;

#define KV_AUX 2
#define W_AUX 0
constexpr int SC1 = 16;
constexpr float QSCALE = 0.12751743082f;
constexpr float THR = 8.0f;

constexpr unsigned WS_EP = 0;
constexpr unsigned WS_FQ = 4096;
constexpr unsigned WS_F1 = WS_FQ + 65536;
constexpr unsigned WS_F2 = WS_F1 + 65536;
constexpr unsigned WS_Q = 262144;
constexpr unsigned WS_PART = WS_Q + 256 * 8192;
constexpr unsigned WS_ML = WS_PART + 1024 * 8192;
constexpr unsigned WS_PO = WS_ML + 1024 * 256;
constexpr unsigned WS_END = WS_PO + 256 * 16384;
constexpr size_t WS_ST = 64u << 20;

constexpr unsigned SLOT = 16384, KRING = 0, VRING = 65536, SPARE = 131072, LDS_BYTES = 163840;
constexpr unsigned QSCR = VRING + 3 * SLOT;
constexpr unsigned CNT = KRING + 3 * SLOT;
constexpr unsigned long long TAGC = 0x9E3779B97F4A7C15ull;
constexpr unsigned SPIN_CAP = 1u << 18;

#define STAMP(i) do {} while (0)
#define LSTAMP(i) do {} while (0)

DI unsigned cvtpk(float lo, float hi) { f32x2 v = {lo, hi}; bf16x2 b = __builtin_convertvector(v, bf16x2); return __builtin_bit_cast(unsigned, b); }
DI bf16x8 asbf(u32x4 v) { return __builtin_bit_cast(bf16x8, v); }
DI int rfl(int x) { return __builtin_amdgcn_readfirstlane(x); }
DI float fmx(float a, float b) { return __builtin_elementwise_maximum(a, b); }
DI float fmx3(float a, float b, float c) { return fmx(fmx(a, b), c); }
DI rsrc_t mkrs(const void* p, unsigned bytes) { return __builtin_amdgcn_make_buffer_rsrc((void*)p, (short)0, bytes, 0x00020000); }
DI void st128(u32x4 v, rsrc_t r, unsigned voff) { __builtin_amdgcn_raw_buffer_store_b128(v, r, voff, 0, SC1); }
DI u32x4 ldsr128(unsigned a) { return *(const LAS u32x4*)(size_t)a; }
DI void ldsw128(unsigned a, u32x4 v) { *(LAS u32x4*)(size_t)a = v; }
DI bf16x8 ldstr(unsigned a) {
  bf16x4 lo = __builtin_amdgcn_ds_read_tr16_b64_v4bf16((LAS bf16x4*)(size_t)a);
  bf16x4 hi = __builtin_amdgcn_ds_read_tr16_b64_v4bf16((LAS bf16x4*)(size_t)(a + 2048));
  return __builtin_shufflevector(lo, hi, 0, 1, 2, 3, 4, 5, 6, 7);
}
#define SBAR() __builtin_amdgcn_sched_barrier(0)
#define BAR() do { SBAR(); asm volatile("s_waitcnt lgkmcnt(0)\n\ts_barrier" ::: "memory"); SBAR(); } while (0)
#define WAITV(n) asm volatile("s_waitcnt vmcnt(%0)" ::"n"(n) : "memory")
#define WAITL() asm volatile("s_waitcnt lgkmcnt(0)" ::: "memory")
#define MFMA32(a, b, c) __builtin_amdgcn_mfma_f32_32x32x16_bf16((a), (b), (c), 0, 0, 0)

DI void poll_flags(const char* base, unsigned stride, int n, unsigned long long tag, int lane) {
  gu64* p = (gu64*)(base + (size_t)(lane < n ? lane : 0) * stride);
#pragma unroll 1
  for (unsigned spins = 0; spins < SPIN_CAP; ++spins) {
    const unsigned long long v = __hip_atomic_load(p, __ATOMIC_RELAXED, __HIP_MEMORY_SCOPE_AGENT);
    if (__all(v == tag)) break;
    __builtin_amdgcn_s_sleep(1);
  }
  asm volatile("" ::: "memory");
}

struct Ld { rsrc_t rk, rv, rw; unsigned vk, vv, vw, lb; int j; };
template <int U> DI void issue(const Ld& L) {
  if constexpr (U < 16) {
    constexpr int t = U >> 1; constexpr bool isv = U & 1;
    const unsigned dst = L.lb + (isv ? VRING : KRING) + (t & 3) * SLOT + L.j * 1024;
#pragma unroll
    for (int i = 0; i < 4; ++i)
      __builtin_amdgcn_raw_ptr_buffer_load_lds(isv ? L.rv : L.rk, (LAS void*)(size_t)(dst + i * 4096), 16, isv ? L.vv : L.vk, t * SLOT + i * 4096, 0, KV_AUX);
  } else {
    constexpr int kt = U - 16;
    constexpr unsigned base = kt == 0 ? SPARE : kt == 1 ? SPARE + SLOT : ((kt & 1) ? VRING : KRING) + ((kt - 2) >> 1) * SLOT;
    const unsigned dst = L.lb + base + L.j * 1024;
#pragma unroll
    for (int i = 0; i < 4; ++i)
      __builtin_amdgcn_raw_ptr_buffer_load_lds(L.rw, (LAS void*)(size_t)(dst + i * 4096), 16, L.vw, (kt * 64 + i * 16) * 1024, 0, W_AUX);
  }
}
constexpr unsigned wo_lds(int kt) { return kt == 0 ? SPARE : kt == 1 ? SPARE + SLOT : ((kt & 1) ? VRING : KRING) + ((kt - 2) >> 1) * SLOT; }
constexpr int last_issued(int j) { return j == 0 ? 5 : j <= 9 ? j + 6 : j <= 12 ? 15 : j + 3; }
template <int J> DI void loader_main(const Ld& L, unsigned long long (&lst)[24]) {
  if constexpr (J < 16) {
    WAITV(4 * (last_issued(J) - J));
    LSTAMP(4 + J);
    BAR();
    if constexpr (J == 0) { issue<6>(L); issue<7>(L); }
    else if constexpr (J <= 8) issue<J + 7>(L);
    else if constexpr (J >= 12 && J <= 14) issue<J + 4>(L);
    else if constexpr (J == 15) { issue<19>(L); issue<20>(L); issue<21>(L); issue<22>(L); issue<23>(L); }
    loader_main<J + 1>(L, lst);
  }
}

template <int W> DI void q_scatter(const f32x4 (&acc)[2][8], f32x4 (&own)[2][2], unsigned scr) {
#pragma unroll
  for (int reg = 0; reg < 4; ++reg)
#pragma unroll
    for (int rh = 0; rh < 2; ++rh)
#pragma unroll
      for (int hf = 0; hf < 2; ++hf) {
        const f32x4 v = {acc[rh][4 * hf][reg], acc[rh][4 * hf + 1][reg], acc[rh][4 * hf + 2][reg], acc[rh][4 * hf + 3][reg]};
        if (reg == W) own[rh][hf] = v;
        else ldsw128(scr + (((reg * 3 + (W < reg ? W : W - 1)) * 2 + rh) * 2 + hf) * 1024, __builtin_bit_cast(u32x4, v));
      }
}

__global__ __launch_bounds__(512, 2) void fused_gqa(const unsigned short* __restrict__ x, const unsigned short* __restrict__ kc, const unsigned short* __restrict__ vc,
                                                    const unsigned short* __restrict__ wq, const unsigned short* __restrict__ wo, unsigned short* __restrict__ out, char* __restrict__ ws) {
  extern __shared__ __attribute__((aligned(16))) char lds[];
  const int bid = blockIdx.x, hkv = bid & 7, b = (bid >> 3) & 7, sp = bid >> 6;
  const int wave = rfl(threadIdx.x >> 6), lane = threadIdx.x & 63;
  const unsigned lb = (unsigned)(size_t)(LAS char*)lds;
  STAMP(0);
  if (wave >= 4) {
    unsigned long long lst[24];
    Ld L; L.j = wave - 4; L.lb = lb;
    const size_t kvoff = ((size_t)(b * 8 + hkv) * 2048 + sp * 512) * 128;
    L.rk = mkrs(kc + kvoff, 512 * 256);
    L.rv = mkrs(vc + kvoff, 512 * 256);
    L.rw = mkrs(wo + (size_t)hkv * 512 * 512 + sp * 128, 512 * 1024 - sp * 256);
    const unsigned r4 = lane >> 4, sl = lane & 15;
    L.vk = L.j * 1024 + r4 * 256 + ((sl ^ (4 * L.j + r4)) * 16);
    L.vv = L.j * 1024 + r4 * 256 + ((sl ^ (r4 << 2)) * 16);
    L.vw = (L.j * 4 + r4) * 1024 + ((sl ^ (r4 << 2)) * 16);
    if (wave == 4 && lane < 2) *(LAS unsigned*)(size_t)(lb + CNT + lane * 4) = 0u;
    BAR();
    issue<0>(L); issue<1>(L); issue<2>(L);
    LSTAMP(0);
    WAITV(8); issue<3>(L);
    LSTAMP(1);
    WAITV(8); issue<4>(L);
    LSTAMP(2);
    WAITV(8); issue<5>(L);
    LSTAMP(3);
    loader_main<0>(L, lst);
    BAR();
    WAITV(0);
    LSTAMP(20);
    BAR();
    BAR();
    return;
  }
  const int w = wave;
  const rsrc_t rws = mkrs(ws, WS_END);
  const unsigned long long ep = __hip_atomic_load((gu64*)(ws + WS_EP + b * 256), __ATOMIC_RELAXED, __HIP_MEMORY_SCOPE_AGENT);
  {
    const int c = lane & 15, g = lane >> 4, h = hkv * 4 + sp;
    const rsrc_t rq = mkrs(wq + h * 128, 512 * 8192 - h * 256);
    const rsrc_t rx = mkrs(x + b * 32 * 512, 32 * 1024);
    const unsigned vq = (128 * w + 8 * g) * 8192 + 16 * c;
    const unsigned vx = c * 1024 + (128 * w + 8 * g) * 2;
    u32x4 wr[4][8], xa[4][2];
#pragma unroll
    for (int st = 0; st < 4; ++st) {
#pragma unroll
      for (int rh = 0; rh < 2; ++rh) xa[st][rh] = __builtin_amdgcn_raw_buffer_load_b128(rx, vx + rh * 16384 + st * 64, 0, W_AUX);
#pragma unroll
      for (int i = 0; i < 8; ++i) wr[st][i] = __builtin_amdgcn_raw_buffer_load_b128(rq, vq, (32 * st + i) * 8192, W_AUX);
    }
    BAR();
    STAMP(1);
    f32x4 acc[2][8];
#pragma unroll
    for (int rh = 0; rh < 2; ++rh)
#pragma unroll
      for (int jj = 0; jj < 8; ++jj) acc[rh][jj] = f32x4{0.f, 0.f, 0.f, 0.f};
#pragma unroll
    for (int st = 0; st < 4; ++st)
#pragma unroll
      for (int jj = 0; jj < 8; ++jj) {
        u32x4 a;
#pragma unroll
        for (int e = 0; e < 4; ++e) a[e] = __builtin_amdgcn_perm(wr[st][2 * e + 1][jj >> 1], wr[st][2 * e][jj >> 1], (jj & 1) ? 0x07060302u : 0x05040100u);
#pragma unroll
        for (int rh = 0; rh < 2; ++rh) acc[rh][jj] = __builtin_amdgcn_mfma_f32_16x16x32_bf16(asbf(a), asbf(xa[st][rh]), acc[rh][jj], 0, 0, 0);
      }
    STAMP(2);
    f32x4 own[2][2];
    switch (w) {
      case 0: q_scatter<0>(acc, own, lb + QSCR + lane * 16); break;
      case 1: q_scatter<1>(acc, own, lb + QSCR + lane * 16); break;
      case 2: q_scatter<2>(acc, own, lb + QSCR + lane * 16); break;
      default: q_scatter<3>(acc, own, lb + QSCR + lane * 16); break;
    }
    WAITL();
    volatile LAS unsigned* c1 = (volatile LAS unsigned*)(size_t)(lb + CNT);
    if (lane == 0) __hip_atomic_fetch_add((LAS unsigned*)c1, 1u, __ATOMIC_RELAXED, __HIP_MEMORY_SCOPE_WORKGROUP);
    while (*c1 < 4u) __builtin_amdgcn_s_sleep(1);
    asm volatile("" ::: "memory");
#pragma unroll
    for (int j = 0; j < 3; ++j)
#pragma unroll
      for (int rh = 0; rh < 2; ++rh)
#pragma unroll
        for (int hf = 0; hf < 2; ++hf) own[rh][hf] += __builtin_bit_cast(f32x4, ldsr128(lb + QSCR + (((w * 3 + j) * 2 + rh) * 2 + hf) * 1024 + lane * 16));
#pragma unroll
    for (int rh = 0; rh < 2; ++rh) {
      u32x4 o;
      o[0] = cvtpk(own[rh][0][0] * QSCALE, own[rh][0][1] * QSCALE);
      o[1] = cvtpk(own[rh][0][2] * QSCALE, own[rh][0][3] * QSCALE);
      o[2] = cvtpk(own[rh][1][0] * QSCALE, own[rh][1][1] * QSCALE);
      o[3] = cvtpk(own[rh][1][2] * QSCALE, own[rh][1][3] * QSCALE);
      st128(o, mkrs(ws + WS_Q + (b * 32 + h) * 8192, 8192), ((2 * g + (w >> 1)) * 64 + (w & 1) * 32 + 16 * rh + c) * 16);
    }
    STAMP(3);
    WAITV(0);
    unsigned prev = 0;
    if (lane == 0) prev = __hip_atomic_fetch_add((LAS unsigned*)(size_t)(lb + CNT + 4), 1u, __ATOMIC_RELAXED, __HIP_MEMORY_SCOPE_WORKGROUP);
    if (lane == 0 && prev == 3u) __hip_atomic_store((gu64*)(ws + WS_FQ + bid * 256), ep + TAGC, __ATOMIC_RELAXED, __HIP_MEMORY_SCOPE_AGENT);
    STAMP(4);
  }
  const unsigned long long tag = ep + TAGC;
  const int grp = hkv + 8 * b;
  poll_flags(ws + WS_FQ + (grp + 64 * w) * 256, 0, 1, tag, lane);
  STAMP(5);
  bf16x8 qf[8];
#pragma unroll
  for (int s = 0; s < 8; ++s) qf[s] = asbf(__builtin_amdgcn_raw_buffer_load_b128(rws, lane * 16 + s * 1024, WS_Q + (b * 32 + hkv * 4 + w) * 8192, SC1));

  const int r = lane & 31, hh = lane >> 5;
  f32x16 o[4], negm;
#pragma unroll
  for (int i = 0; i < 16; ++i) { o[0][i] = 0.f; o[1][i] = 0.f; o[2][i] = 0.f; o[3][i] = 0.f; negm[i] = 0.f; }
  float m = 0.f, l0 = 0.f, l1 = 0.f;
  const unsigned kbase = lb + KRING + 256 * r + 16 * (hh ^ (r & 15));
  const int q4 = (lane & 15) >> 2, p4 = lane & 3, blk = (lane >> 4) & 1;
  const unsigned vlane = 256 * (4 * hh + q4) + 32 * blk + 8 * p4;
  WAITV(0);
  STAMP(6);
  for (int t = 0; t < 8; ++t) {
    const unsigned so = (t & 3) * SLOT;
    BAR();
    STAMP(8 + 2 * t);
    f32x16 s0, s1;
    u32x4 kf[16];
    unsigned ka[8];
#pragma unroll
    for (int s = 0; s < 8; ++s) ka[s] = (kbase ^ (s << 5)) + so;
#pragma unroll
    for (int i = 0; i < 6; ++i) kf[i] = ldsr128(ka[i]);
    SBAR();
    float pm0 = 0.f, pm1;
#pragma unroll
    for (int mi = 0; mi < 16; ++mi) {
      if (mi == 0) s0 = MFMA32(asbf(kf[0]), qf[0], negm);
      else if (mi < 8) s0 = MFMA32(asbf(kf[mi]), qf[mi], s0);
      else if (mi == 8) s1 = MFMA32(asbf(kf[8]), qf[0], negm);
      else s1 = MFMA32(asbf(kf[mi]), qf[mi - 8], s1);
      if (mi + 6 < 16) kf[mi + 6] = ldsr128(ka[(mi + 6) & 7] + ((mi + 6) >> 3) * 8192);
      if (mi == 11) { pm0 = fmx3(s0[0], s0[1], s0[2]); pm0 = fmx3(pm0, s0[3], s0[4]); }
      if (mi == 12) { pm0 = fmx3(pm0, s0[5], s0[6]); pm0 = fmx3(pm0, s0[7], s0[8]); }
      if (mi == 13) { pm0 = fmx3(pm0, s0[9], s0[10]); pm0 = fmx3(pm0, s0[11], s0[12]); }
      if (mi == 14) { pm0 = fmx3(pm0, s0[13], s0[14]); pm0 = fmx(pm0, s0[15]); }
      SBAR();
    }
    pm1 = fmx3(s1[0], s1[1], s1[2]);
#pragma unroll
    for (int i = 3; i < 15; i += 2) pm1 = fmx3(pm1, s1[i], s1[i + 1]);
    float pm = fmx3(pm0, pm1, s1[15]);
    { auto rr = __builtin_amdgcn_permlane32_swap(__float_as_uint(pm), __float_as_uint(pm), false, false);
      pm = fmx(__uint_as_float(rr[0]), __uint_as_float(rr[1])); }
    if (t == 0 || __any(pm > THR)) {
      const float dl = t == 0 ? pm : fmx(pm, 0.f);
      const float alpha = t == 0 ? 1.f : __builtin_amdgcn_exp2f(-dl);
      m += dl;
#pragma unroll
      for (int i = 0; i < 16; ++i) { s0[i] -= dl; s1[i] -= dl; negm[i] = -m; }
      l0 *= alpha; l1 *= alpha;
#pragma unroll
      for (int db = 0; db < 4; ++db)
#pragma unroll
        for (int i = 0; i < 16; ++i) o[db][i] *= alpha;
    }
    SBAR();
    bf16x8 pf[4];
    {
      u32x4 a, c, d, e;
#pragma unroll
      for (int i = 0; i < 16; ++i) { s0[i] = __builtin_amdgcn_exp2f(s0[i]); s1[i] = __builtin_amdgcn_exp2f(s1[i]); l0 += s0[i]; l1 += s1[i]; }
#pragma unroll
      for (int i = 0; i < 4; ++i) {
        a[i] = cvtpk(s0[2 * i], s0[2 * i + 1]); c[i] = cvtpk(s0[8 + 2 * i], s0[8 + 2 * i + 1]);
        d[i] = cvtpk(s1[2 * i], s1[2 * i + 1]); e[i] = cvtpk(s1[8 + 2 * i], s1[8 + 2 * i + 1]);
      }
      pf[0] = asbf(a); pf[1] = asbf(c); pf[2] = asbf(d); pf[3] = asbf(e);
      asm volatile("" : "+v"(a), "+v"(c), "+v"(d), "+v"(e));
      pf[0] = asbf(a); pf[1] = asbf(c); pf[2] = asbf(d); pf[3] = asbf(e);
    }
    BAR();
    STAMP(9 + 2 * t);
    unsigned va[4];
#pragma unroll
    for (int db = 0; db < 4; ++db) va[db] = lb + VRING + so + vlane + 64 * (db ^ q4);
    bf16x8 vf[16];
#pragma unroll
    for (int i = 0; i < 5; ++i) vf[i] = ldstr(va[i & 3] + (i >> 2) * 4096);
    SBAR();
#pragma unroll
    for (int mi = 0; mi < 16; ++mi) {
      o[mi & 3] = MFMA32(vf[mi], pf[mi >> 2], o[mi & 3]);
      if (mi + 5 < 16) vf[mi + 5] = ldstr(va[(mi + 5) & 3] + ((mi + 5) >> 2) * 4096);
      SBAR();
    }
  }
  STAMP(24);
  float l = l0 + l1;
  { auto rr = __builtin_amdgcn_permlane32_swap(__float_as_uint(l), __float_as_uint(l), false, false);
    l = __uint_as_float(rr[0]) + __uint_as_float(rr[1]); }
  const int pgrp = (b * 8 + hkv) * 4;
  {
    const rsrc_t rp = mkrs(ws + WS_PART + ((pgrp + sp) * 4 + w) * 8192, 8192);
#pragma unroll
    for (int db = 0; db < 4; ++db)
#pragma unroll
      for (int sx = 0; sx < 2; ++sx) {
        u32x4 v;
#pragma unroll
        for (int i = 0; i < 4; ++i) v[i] = cvtpk(o[db][8 * sx + 2 * i], o[db][8 * sx + 2 * i + 1]);
        st128(v, rp, lane * 16 + (2 * db + sx) * 1024);
      }
    if (hh == 0) __builtin_amdgcn_raw_buffer_store_b64(u32x2{__float_as_uint(m), __float_as_uint(l)}, rws, r * 8, WS_ML + ((pgrp + sp) * 4 + w) * 256, SC1);
  }
  STAMP(25);
  WAITV(0);
  STAMP(26);
  BAR();
  if (w == 0 && lane == 0) __hip_atomic_store((gu64*)(ws + WS_F1 + bid * 256), tag, __ATOMIC_RELAXED, __HIP_MEMORY_SCOPE_AGENT);
  poll_flags(ws + WS_F1 + grp * 256, 64 * 256, 4, tag, lane);
  STAMP(27);
  {
    u32x2 st[3]; u32x4 pr[3][8];
#pragma unroll
    for (int j = 0; j < 3; ++j) {
      const int s2 = (sp + 1 + j) & 3;
      st[j] = __builtin_amdgcn_raw_buffer_load_b64(rws, r * 8, WS_ML + ((pgrp + s2) * 4 + w) * 256, SC1);
    }
#pragma unroll
    for (int j = 0; j < 3; ++j) {
      const int s2 = (sp + 1 + j) & 3;
#pragma unroll
      for (int f = 0; f < 8; ++f) pr[j][f] = __builtin_amdgcn_raw_buffer_load_b128(rws, lane * 16 + f * 1024, WS_PART + ((pgrp + s2) * 4 + w) * 8192, SC1);
    }
    float M = m;
#pragma unroll
    for (int j = 0; j < 3; ++j) M = fmx(M, __uint_as_float(st[j][0]));
    float a0 = __builtin_amdgcn_exp2f(m - M), a[3];
    float L = a0 * l;
#pragma unroll
    for (int j = 0; j < 3; ++j) { a[j] = __builtin_amdgcn_exp2f(__uint_as_float(st[j][0]) - M); L += a[j] * __uint_as_float(st[j][1]); }
    const float inv = 1.f / L;
    a0 *= inv;
#pragma unroll
    for (int j = 0; j < 3; ++j) a[j] *= inv;
    const unsigned ofr = lb + (w < 2 ? KRING + 3 * SLOT + w * 8192 : VRING + 3 * SLOT + (w - 2) * 8192) + lane * 16;
#pragma unroll
    for (int f = 0; f < 8; ++f) {
      u32x4 ov;
#pragma unroll
      for (int i = 0; i < 4; ++i) {
        float lo = a0 * o[f >> 1][8 * (f & 1) + 2 * i], hi = a0 * o[f >> 1][8 * (f & 1) + 2 * i + 1];
#pragma unroll
        for (int j = 0; j < 3; ++j) { lo += a[j] * __uint_as_float(pr[j][f][i] << 16); hi += a[j] * __uint_as_float(pr[j][f][i] & 0xffff0000u); }
        ov[i] = cvtpk(lo, hi);
      }
      ldsw128(ofr + f * 1024, ov);
    }
  }
  STAMP(28);
  BAR();
  STAMP(29);
  {
    const unsigned wl = lb + vlane + 64 * (w ^ q4);
    f32x16 acc;
#pragma unroll
    for (int i = 0; i < 16; ++i) acc[i] = 0.f;
#pragma unroll
    for (int kt = 0; kt < 8; ++kt)
#pragma unroll
      for (int ks = 0; ks < 4; ++ks) {
        const int hd = kt >> 1, f = 4 * (kt & 1) + ks;
        const bf16x8 wf = ldstr(wl + wo_lds(kt) + 4096 * ks);
        const bf16x8 of = asbf(ldsr128(lb + (hd < 2 ? KRING + 3 * SLOT + hd * 8192 : VRING + 3 * SLOT + (hd - 2) * 8192) + f * 1024 + lane * 16));
        acc = MFMA32(wf, of, acc);
      }
    const rsrc_t rpo = mkrs(ws + WS_PO + ((b * 4 + sp) * 8 + hkv) * 16384 + w * 4096, 4096);
#pragma unroll
    for (int q = 0; q < 4; ++q) {
      f32x4 v = {acc[4 * q], acc[4 * q + 1], acc[4 * q + 2], acc[4 * q + 3]};
      st128(__builtin_bit_cast(u32x4, v), rpo, lane * 16 + q * 1024);
    }
  }
  STAMP(30);
  WAITV(0);
  STAMP(31);
  BAR();
  if (w == 0 && lane == 0) __hip_atomic_store((gu64*)(ws + WS_F2 + bid * 256), tag, __ATOMIC_RELAXED, __HIP_MEMORY_SCOPE_AGENT);
  if (w < 2) {
    poll_flags(ws + WS_F2 + (8 * b + 64 * sp) * 256, 256, 8, tag, lane);
    STAMP(32);
    const int pc = 8 * w + (lane >> 3), sub = lane & 7, h2 = sub >> 2, row = 4 * hkv + (sub & 3);
    u32x4 pv[8];
#pragma unroll
    for (int k = 0; k < 8; ++k) pv[k] = __builtin_amdgcn_raw_buffer_load_b128(rws, pc * 1024 + (h2 * 32 + row) * 16, WS_PO + ((b * 4 + sp) * 8 + k) * 16384, SC1);
    f32x4 s = __builtin_bit_cast(f32x4, pv[0]);
#pragma unroll
    for (int k = 1; k < 8; ++k) s += __builtin_bit_cast(f32x4, pv[k]);
    const int n = 32 * (pc >> 2) + 8 * (pc & 3) + 4 * h2;
    *(u32x2*)(out + (size_t)(b * 32 + row) * 512 + sp * 128 + n) = u32x2{cvtpk(s[0], s[1]), cvtpk(s[2], s[3])};
    if (w == 0 && hkv == 0 && sp == 0 && lane == 0) __hip_atomic_store((gu64*)(ws + WS_EP + b * 256), ep + 1ull, __ATOMIC_RELAXED, __HIP_MEMORY_SCOPE_AGENT);
    STAMP(33);
  }
}

extern "C" void kernel_launch(void* const* d_in, const int* in_sizes, int n_in, void* d_out, int out_size, void* d_ws, size_t ws_size, hipStream_t stream) {
  (void)hipFuncSetAttribute((const void*)fused_gqa, hipFuncAttributeMaxDynamicSharedMemorySize, LDS_BYTES);
  fused_gqa<<<256, 512, LDS_BYTES, stream>>>((const unsigned short*)d_in[0], (const unsigned short*)d_in[1], (const unsigned short*)d_in[2],
                                             (const unsigned short*)d_in[3], (const unsigned short*)d_in[4], (unsigned short*)d_out, (char*)d_ws);
}
```

```cpp
#define DIAG 24
#define DIAG_BID 9
#define DIAG_MODE 2
#define DIAG_THR 21.000000f
#include <hip/hip_runtime.h>
#include <hip/hip_bf16.h>
#include <stdint.h>

typedef __attribute__((ext_vector_type(8))) __bf16 bf16x8;
typedef __attribute__((ext_vector_type(4))) __bf16 bf16x4;
typedef __attribute__((ext_vector_type(2))) __bf16 bf16x2;
typedef __attribute__((ext_vector_type(16))) float f32x16;
typedef __attribute__((ext_vector_type(4))) float f32x4;
typedef __attribute__((ext_vector_type(2))) float f32x2;
typedef __attribute__((ext_vector_type(4))) unsigned u32x4;
typedef __attribute__((ext_vector_type(2))) unsigned u32x2;
#define LAS __attribute__((address_space(3)))
#define GAS __attribute__((address_space(1)))
#define DI __device__ __forceinline__
typedef GAS unsigned long long gu64;
typedef __amdgpu_buffer_rsrc_t rsrc_t;

#define KV_AUX 2
#define W_AUX 0
constexpr int SC1 = 16;
constexpr float QSCALE = 0.12751743082f;
constexpr float THR = 8.0f;

constexpr unsigned WS_EP = 0;
constexpr unsigned WS_FQ = 4096;
constexpr unsigned WS_F1 = WS_FQ + 65536;
constexpr unsigned WS_F2 = WS_F1 + 65536;
constexpr unsigned WS_Q = 262144;
constexpr unsigned WS_PART = WS_Q + 256 * 8192;
constexpr unsigned WS_ML = WS_PART + 1024 * 8192;
constexpr unsigned WS_PO = WS_ML + 1024 * 256;
constexpr unsigned WS_END = WS_PO + 256 * 16384;
constexpr size_t WS_ST = 64u << 20;

constexpr unsigned SLOT = 16384, KRING = 0, VRING = 65536, SPARE = 131072, LDS_BYTES = 163840;
constexpr unsigned QSCR = VRING + 3 * SLOT;
constexpr unsigned CNT = KRING + 3 * SLOT;
constexpr unsigned long long TAGC = 0x9E3779B97F4A7C15ull;
constexpr unsigned SPIN_CAP = 1u << 18;

#define STAMP(i) do { if ((i) == 0) dg0 = __builtin_amdgcn_s_memrealtime(); if ((i) == DIAG) dg1 = __builtin_amdgcn_s_memrealtime(); } while (0)
#define LSTAMP(i) do {} while (0)

DI unsigned cvtpk(float lo, float hi) { f32x2 v = {lo, hi}; bf16x2 b = __builtin_convertvector(v, bf16x2); return __builtin_bit_cast(unsigned, b); }
DI bf16x8 asbf(u32x4 v) { return __builtin_bit_cast(bf16x8, v); }
DI int rfl(int x) { return __builtin_amdgcn_readfirstlane(x); }
DI float fmx(float a, float b) { return __builtin_elementwise_maximum(a, b); }
DI float fmx3(float a, float b, float c) { return fmx(fmx(a, b), c); }
DI rsrc_t mkrs(const void* p, unsigned bytes) { return __builtin_amdgcn_make_buffer_rsrc((void*)p, (short)0, bytes, 0x00020000); }
DI void st128(u32x4 v, rsrc_t r, unsigned voff) { __builtin_amdgcn_raw_buffer_store_b128(v, r, voff, 0, SC1); }
DI u32x4 ldsr128(unsigned a) { return *(const LAS u32x4*)(size_t)a; }
DI void ldsw128(unsigned a, u32x4 v) { *(LAS u32x4*)(size_t)a = v; }
DI bf16x8 ldstr(unsigned a) {
  bf16x4 lo = __builtin_amdgcn_ds_read_tr16_b64_v4bf16((LAS bf16x4*)(size_t)a);
  bf16x4 hi = __builtin_amdgcn_ds_read_tr16_b64_v4bf16((LAS bf16x4*)(size_t)(a + 2048));
  return __builtin_shufflevector(lo, hi, 0, 1, 2, 3, 4, 5, 6, 7);
}
#define SBAR() __builtin_amdgcn_sched_barrier(0)
#define BAR() do { SBAR(); asm volatile("s_waitcnt lgkmcnt(0)\n\ts_barrier" ::: "memory"); SBAR(); } while (0)
#define WAITV(n) asm volatile("s_waitcnt vmcnt(%0)" ::"n"(n) : "memory")
#define WAITL() asm volatile("s_waitcnt lgkmcnt(0)" ::: "memory")
#define MFMA32(a, b, c) __builtin_amdgcn_mfma_f32_32x32x16_bf16((a), (b), (c), 0, 0, 0)

DI void poll_flags(const char* base, unsigned stride, int n, unsigned long long tag, int lane) {
  gu64* p = (gu64*)(base + (size_t)(lane < n ? lane : 0) * stride);
#pragma unroll 1
  for (unsigned spins = 0; spins < SPIN_CAP; ++spins) {
    const unsigned long long v = __hip_atomic_load(p, __ATOMIC_RELAXED, __HIP_MEMORY_SCOPE_AGENT);
    if (__all(v == tag)) break;
    __builtin_amdgcn_s_sleep(1);
  }
  asm volatile("" ::: "memory");
}

struct Ld { rsrc_t rk, rv, rw; unsigned vk, vv, vw, lb; int j; };
template <int U> DI void issue(const Ld& L) {
  if constexpr (U < 16) {
    constexpr int t = U >> 1; constexpr bool isv = U & 1;
    const unsigned dst = L.lb + (isv ? VRING : KRING) + (t & 3) * SLOT + L.j * 1024;
#pragma unroll
    for (int i = 0; i < 4; ++i)
      __builtin_amdgcn_raw_ptr_buffer_load_lds(isv ? L.rv : L.rk, (LAS void*)(size_t)(dst + i * 4096), 16, isv ? L.vv : L.vk, t * SLOT + i * 4096, 0, KV_AUX);
  } else {
    constexpr int kt = U - 16;
    constexpr unsigned base = kt == 0 ? SPARE : kt == 1 ? SPARE + SLOT : ((kt & 1) ? VRING : KRING) + ((kt - 2) >> 1) * SLOT;
    const unsigned dst = L.lb + base + L.j * 1024;
#pragma unroll
    for (int i = 0; i < 4; ++i)
      __builtin_amdgcn_raw_ptr_buffer_load_lds(L.rw, (LAS void*)(size_t)(dst + i * 4096), 16, L.vw, (kt * 64 + i * 16) * 1024, 0, W_AUX);
  }
}
constexpr unsigned wo_lds(int kt) { return kt == 0 ? SPARE : kt == 1 ? SPARE + SLOT : ((kt & 1) ? VRING : KRING) + ((kt - 2) >> 1) * SLOT; }
constexpr int last_issued(int j) { return j == 0 ? 5 : j <= 9 ? j + 6 : j <= 12 ? 15 : j + 3; }
template <int J> DI void loader_main(const Ld& L, unsigned long long (&lst)[24]) {
  if constexpr (J < 16) {
    WAITV(4 * (last_issued(J) - J));
    LSTAMP(4 + J);
    BAR();
    if constexpr (J == 0) { issue<6>(L); issue<7>(L); }
    else if constexpr (J <= 8) issue<J + 7>(L);
    else if constexpr (J >= 12 && J <= 14) issue<J + 4>(L);
    else if constexpr (J == 15) { issue<19>(L); issue<20>(L); issue<21>(L); issue<22>(L); issue<23>(L); }
    loader_main<J + 1>(L, lst);
  }
}

template <int W> DI void q_scatter(const f32x4 (&acc)[2][8], f32x4 (&own)[2][2], unsigned scr) {
#pragma unroll
  for (int reg = 0; reg < 4; ++reg)
#pragma unroll
    for (int rh = 0; rh < 2; ++rh)
#pragma unroll
      for (int hf = 0; hf < 2; ++hf) {
        const f32x4 v = {acc[rh][4 * hf][reg], acc[rh][4 * hf + 1][reg], acc[rh][4 * hf + 2][reg], acc[rh][4 * hf + 3][reg]};
        if (reg == W) own[rh][hf] = v;
        else ldsw128(scr + (((reg * 3 + (W < reg ? W : W - 1)) * 2 + rh) * 2 + hf) * 1024, __builtin_bit_cast(u32x4, v));
      }
}

__global__ __launch_bounds__(512, 2) void fused_gqa(const unsigned short* __restrict__ x, const unsigned short* __restrict__ kc, const unsigned short* __restrict__ vc,
                                                    const unsigned short* __restrict__ wq, const unsigned short* __restrict__ wo, unsigned short* __restrict__ out, char* __restrict__ ws) {
  extern __shared__ __attribute__((aligned(16))) char lds[];
  const int bid = blockIdx.x, hkv = bid & 7, b = (bid >> 3) & 7, sp = bid >> 6;
  const int wave = rfl(threadIdx.x >> 6), lane = threadIdx.x & 63;
  const unsigned lb = (unsigned)(size_t)(LAS char*)lds;
  unsigned long long dg0 = 0, dg1 = 0;
  STAMP(0);
  if (wave >= 4) {
    unsigned long long lst[24];
    Ld L; L.j = wave - 4; L.lb = lb;
    const size_t kvoff = ((size_t)(b * 8 + hkv) * 2048 + sp * 512) * 128;
    L.rk = mkrs(kc + kvoff, 512 * 256);
    L.rv = mkrs(vc + kvoff, 512 * 256);
    L.rw = mkrs(wo + (size_t)hkv * 512 * 512 + sp * 128, 512 * 1024 - sp * 256);
    const unsigned r4 = lane >> 4, sl = lane & 15;
    L.vk = L.j * 1024 + r4 * 256 + ((sl ^ (4 * L.j + r4)) * 16);
    L.vv = L.j * 1024 + r4 * 256 + ((sl ^ (r4 << 2)) * 16);
    L.vw = (L.j * 4 + r4) * 1024 + ((sl ^ (r4 << 2)) * 16);
    if (wave == 4 && lane < 2) *(LAS unsigned*)(size_t)(lb + CNT + lane * 4) = 0u;
    BAR();
    issue<0>(L); issue<1>(L); issue<2>(L);
    LSTAMP(0);
    WAITV(8); issue<3>(L);
    LSTAMP(1);
    WAITV(8); issue<4>(L);
    LSTAMP(2);
    WAITV(8); issue<5>(L);
    LSTAMP(3);
    loader_main<0>(L, lst);
    BAR();
    WAITV(0);
    LSTAMP(20);
    BAR();
    BAR();
    return;
  }
  const int w = wave;
  const rsrc_t rws = mkrs(ws, WS_END);
  if (w == 0 && lane == 0) __hip_atomic_store((gu64*)(ws + (32u << 20) + bid * 8), dg0, __ATOMIC_RELAXED, __HIP_MEMORY_SCOPE_AGENT);
  const unsigned long long ep = __hip_atomic_load((gu64*)(ws + WS_EP + b * 256), __ATOMIC_RELAXED, __HIP_MEMORY_SCOPE_AGENT);
  {
    const int c = lane & 15, g = lane >> 4, h = hkv * 4 + sp;
    const rsrc_t rq = mkrs(wq + h * 128, 512 * 8192 - h * 256);
    const rsrc_t rx = mkrs(x + b * 32 * 512, 32 * 1024);
    const unsigned vq = (128 * w + 8 * g) * 8192 + 16 * c;
    const unsigned vx = c * 1024 + (128 * w + 8 * g) * 2;
    u32x4 wr[4][8], xa[4][2];
#pragma unroll
    for (int st = 0; st < 4; ++st) {
#pragma unroll
      for (int rh = 0; rh < 2; ++rh) xa[st][rh] = __builtin_amdgcn_raw_buffer_load_b128(rx, vx + rh * 16384 + st * 64, 0, W_AUX);
#pragma unroll
      for (int i = 0; i < 8; ++i) wr[st][i] = __builtin_amdgcn_raw_buffer_load_b128(rq, vq, (32 * st + i) * 8192, W_AUX);
    }
    BAR();
    STAMP(1);
    f32x4 acc[2][8];
#pragma unroll
    for (int rh = 0; rh < 2; ++rh)
#pragma unroll
      for (int jj = 0; jj < 8; ++jj) acc[rh][jj] = f32x4{0.f, 0.f, 0.f, 0.f};
#pragma unroll
    for (int st = 0; st < 4; ++st)
#pragma unroll
      for (int jj = 0; jj < 8; ++jj) {
        u32x4 a;
#pragma unroll
        for (int e = 0; e < 4; ++e) a[e] = __builtin_amdgcn_perm(wr[st][2 * e + 1][jj >> 1], wr[st][2 * e][jj >> 1], (jj & 1) ? 0x07060302u : 0x05040100u);
#pragma unroll
        for (int rh = 0; rh < 2; ++rh) acc[rh][jj] = __builtin_amdgcn_mfma_f32_16x16x32_bf16(asbf(a), asbf(xa[st][rh]), acc[rh][jj], 0, 0, 0);
      }
    STAMP(2);
    f32x4 own[2][2];
    switch (w) {
      case 0: q_scatter<0>(acc, own, lb + QSCR + lane * 16); break;
      case 1: q_scatter<1>(acc, own, lb + QSCR + lane * 16); break;
      case 2: q_scatter<2>(acc, own, lb + QSCR + lane * 16); break;
      default: q_scatter<3>(acc, own, lb + QSCR + lane * 16); break;
    }
    WAITL();
    volatile LAS unsigned* c1 = (volatile LAS unsigned*)(size_t)(lb + CNT);
    if (lane == 0) __hip_atomic_fetch_add((LAS unsigned*)c1, 1u, __ATOMIC_RELAXED, __HIP_MEMORY_SCOPE_WORKGROUP);
    while (*c1 < 4u) __builtin_amdgcn_s_sleep(1);
    asm volatile("" ::: "memory");
#pragma unroll
    for (int j = 0; j < 3; ++j)
#pragma unroll
      for (int rh = 0; rh < 2; ++rh)
#pragma unroll
        for (int hf = 0; hf < 2; ++hf) own[rh][hf] += __builtin_bit_cast(f32x4, ldsr128(lb + QSCR + (((w * 3 + j) * 2 + rh) * 2 + hf) * 1024 + lane * 16));
#pragma unroll
    for (int rh = 0; rh < 2; ++rh) {
      u32x4 o;
      o[0] = cvtpk(own[rh][0][0] * QSCALE, own[rh][0][1] * QSCALE);
      o[1] = cvtpk(own[rh][0][2] * QSCALE, own[rh][0][3] * QSCALE);
      o[2] = cvtpk(own[rh][1][0] * QSCALE, own[rh][1][1] * QSCALE);
      o[3] = cvtpk(own[rh][1][2] * QSCALE, own[rh][1][3] * QSCALE);
      st128(o, mkrs(ws + WS_Q + (b * 32 + h) * 8192, 8192), ((2 * g + (w >> 1)) * 64 + (w & 1) * 32 + 16 * rh + c) * 16);
    }
    STAMP(3);
    WAITV(0);
    unsigned prev = 0;
    if (lane == 0) prev = __hip_atomic_fetch_add((LAS unsigned*)(size_t)(lb + CNT + 4), 1u, __ATOMIC_RELAXED, __HIP_MEMORY_SCOPE_WORKGROUP);
    if (lane == 0 && prev == 3u) __hip_atomic_store((gu64*)(ws + WS_FQ + bid * 256), ep + TAGC, __ATOMIC_RELAXED, __HIP_MEMORY_SCOPE_AGENT);
    STAMP(4);
  }
  const unsigned long long tag = ep + TAGC;
  const int grp = hkv + 8 * b;
  poll_flags(ws + WS_FQ + (grp + 64 * w) * 256, 0, 1, tag, lane);
  STAMP(5);
  bf16x8 qf[8];
#pragma unroll
  for (int s = 0; s < 8; ++s) qf[s] = asbf(__builtin_amdgcn_raw_buffer_load_b128(rws, lane * 16 + s * 1024, WS_Q + (b * 32 + hkv * 4 + w) * 8192, SC1));

  const int r = lane & 31, hh = lane >> 5;
  f32x16 o[4], negm;
#pragma unroll
  for (int i = 0; i < 16; ++i) { o[0][i] = 0.f; o[1][i] = 0.f; o[2][i] = 0.f; o[3][i] = 0.f; negm[i] = 0.f; }
  float m = 0.f, l0 = 0.f, l1 = 0.f;
  const unsigned kbase = lb + KRING + 256 * r + 16 * (hh ^ (r & 15));
  const int q4 = (lane & 15) >> 2, p4 = lane & 3, blk = (lane >> 4) & 1;
  const unsigned vlane = 256 * (4 * hh + q4) + 32 * blk + 8 * p4;
  WAITV(0);
  STAMP(6);
  for (int t = 0; t < 8; ++t) {
    const unsigned so = (t & 3) * SLOT;
    BAR();
    STAMP(8 + 2 * t);
    f32x16 s0, s1;
    u32x4 kf[16];
    unsigned ka[8];
#pragma unroll
    for (int s = 0; s < 8; ++s) ka[s] = (kbase ^ (s << 5)) + so;
#pragma unroll
    for (int i = 0; i < 6; ++i) kf[i] = ldsr128(ka[i]);
    SBAR();
    float pm0 = 0.f, pm1;
#pragma unroll
    for (int mi = 0; mi < 16; ++mi) {
      if (mi == 0) s0 = MFMA32(asbf(kf[0]), qf[0], negm);
      else if (mi < 8) s0 = MFMA32(asbf(kf[mi]), qf[mi], s0);
      else if (mi == 8) s1 = MFMA32(asbf(kf[8]), qf[0], negm);
      else s1 = MFMA32(asbf(kf[mi]), qf[mi - 8], s1);
      if (mi + 6 < 16) kf[mi + 6] = ldsr128(ka[(mi + 6) & 7] + ((mi + 6) >> 3) * 8192);
      if (mi == 11) { pm0 = fmx3(s0[0], s0[1], s0[2]); pm0 = fmx3(pm0, s0[3], s0[4]); }
      if (mi == 12) { pm0 = fmx3(pm0, s0[5], s0[6]); pm0 = fmx3(pm0, s0[7], s0[8]); }
      if (mi == 13) { pm0 = fmx3(pm0, s0[9], s0[10]); pm0 = fmx3(pm0, s0[11], s0[12]); }
      if (mi == 14) { pm0 = fmx3(pm0, s0[13], s0[14]); pm0 = fmx(pm0, s0[15]); }
      SBAR();
    }
    pm1 = fmx3(s1[0], s1[1], s1[2]);
#pragma unroll
    for (int i = 3; i < 15; i += 2) pm1 = fmx3(pm1, s1[i], s1[i + 1]);
    float pm = fmx3(pm0, pm1, s1[15]);
    { auto rr = __builtin_amdgcn_permlane32_swap(__float_as_uint(pm), __float_as_uint(pm), false, false);
      pm = fmx(__uint_as_float(rr[0]), __uint_as_float(rr[1])); }
    if (t == 0 || __any(pm > THR)) {
      const float dl = t == 0 ? pm : fmx(pm, 0.f);
      const float alpha = t == 0 ? 1.f : __builtin_amdgcn_exp2f(-dl);
      m += dl;
#pragma unroll
      for (int i = 0; i < 16; ++i) { s0[i] -= dl; s1[i] -= dl; negm[i] = -m; }
      l0 *= alpha; l1 *= alpha;
#pragma unroll
      for (int db = 0; db < 4; ++db)
#pragma unroll
        for (int i = 0; i < 16; ++i) o[db][i] *= alpha;
    }
    SBAR();
    bf16x8 pf[4];
    {
      u32x4 a, c, d, e;
#pragma unroll
      for (int i = 0; i < 16; ++i) { s0[i] = __builtin_amdgcn_exp2f(s0[i]); s1[i] = __builtin_amdgcn_exp2f(s1[i]); l0 += s0[i]; l1 += s1[i]; }
#pragma unroll
      for (int i = 0; i < 4; ++i) {
        a[i] = cvtpk(s0[2 * i], s0[2 * i + 1]); c[i] = cvtpk(s0[8 + 2 * i], s0[8 + 2 * i + 1]);
        d[i] = cvtpk(s1[2 * i], s1[2 * i + 1]); e[i] = cvtpk(s1[8 + 2 * i], s1[8 + 2 * i + 1]);
      }
      pf[0] = asbf(a); pf[1] = asbf(c); pf[2] = asbf(d); pf[3] = asbf(e);
      asm volatile("" : "+v"(a), "+v"(c), "+v"(d), "+v"(e));
      pf[0] = asbf(a); pf[1] = asbf(c); pf[2] = asbf(d); pf[3] = asbf(e);
    }
    BAR();
    STAMP(9 + 2 * t);
    unsigned va[4];
#pragma unroll
    for (int db = 0; db < 4; ++db) va[db] = lb + VRING + so + vlane + 64 * (db ^ q4);
    bf16x8 vf[16];
#pragma unroll
    for (int i = 0; i < 5; ++i) vf[i] = ldstr(va[i & 3] + (i >> 2) * 4096);
    SBAR();
#pragma unroll
    for (int mi = 0; mi < 16; ++mi) {
      o[mi & 3] = MFMA32(vf[mi], pf[mi >> 2], o[mi & 3]);
      if (mi + 5 < 16) vf[mi + 5] = ldstr(va[(mi + 5) & 3] + ((mi + 5) >> 2) * 4096);
      SBAR();
    }
  }
  STAMP(24);
  float l = l0 + l1;
  { auto rr = __builtin_amdgcn_permlane32_swap(__float_as_uint(l), __float_as_uint(l), false, false);
    l = __uint_as_float(rr[0]) + __uint_as_float(rr[1]); }
  const int pgrp = (b * 8 + hkv) * 4;
  {
    const rsrc_t rp = mkrs(ws + WS_PART + ((pgrp + sp) * 4 + w) * 8192, 8192);
#pragma unroll
    for (int db = 0; db < 4; ++db)
#pragma unroll
      for (int sx = 0; sx < 2; ++sx) {
        u32x4 v;
#pragma unroll
        for (int i = 0; i < 4; ++i) v[i] = cvtpk(o[db][8 * sx + 2 * i], o[db][8 * sx + 2 * i + 1]);
        st128(v, rp, lane * 16 + (2 * db + sx) * 1024);
      }
    if (hh == 0) __builtin_amdgcn_raw_buffer_store_b64(u32x2{__float_as_uint(m), __float_as_uint(l)}, rws, r * 8, WS_ML + ((pgrp + sp) * 4 + w) * 256, SC1);
  }
  STAMP(25);
  WAITV(0);
  STAMP(26);
  BAR();
  if (w == 0 && lane == 0) __hip_atomic_store((gu64*)(ws + WS_F1 + bid * 256), tag, __ATOMIC_RELAXED, __HIP_MEMORY_SCOPE_AGENT);
  poll_flags(ws + WS_F1 + grp * 256, 64 * 256, 4, tag, lane);
  STAMP(27);
  {
    u32x2 st[3]; u32x4 pr[3][8];
#pragma unroll
    for (int j = 0; j < 3; ++j) {
      const int s2 = (sp + 1 + j) & 3;
      st[j] = __builtin_amdgcn_raw_buffer_load_b64(rws, r * 8, WS_ML + ((pgrp + s2) * 4 + w) * 256, SC1);
    }
#pragma unroll
    for (int j = 0; j < 3; ++j) {
      const int s2 = (sp + 1 + j) & 3;
#pragma unroll
      for (int f = 0; f < 8; ++f) pr[j][f] = __builtin_amdgcn_raw_buffer_load_b128(rws, lane * 16 + f * 1024, WS_PART + ((pgrp + s2) * 4 + w) * 8192, SC1);
    }
    float M = m;
#pragma unroll
    for (int j = 0; j < 3; ++j) M = fmx(M, __uint_as_float(st[j][0]));
    float a0 = __builtin_amdgcn_exp2f(m - M), a[3];
    float L = a0 * l;
#pragma unroll
    for (int j = 0; j < 3; ++j) { a[j] = __builtin_amdgcn_exp2f(__uint_as_float(st[j][0]) - M); L += a[j] * __uint_as_float(st[j][1]); }
    const float inv = 1.f / L;
    a0 *= inv;
#pragma unroll
    for (int j = 0; j < 3; ++j) a[j] *= inv;
    const unsigned ofr = lb + (w < 2 ? KRING + 3 * SLOT + w * 8192 : VRING + 3 * SLOT + (w - 2) * 8192) + lane * 16;
#pragma unroll
    for (int f = 0; f < 8; ++f) {
      u32x4 ov;
#pragma unroll
      for (int i = 0; i < 4; ++i) {
        float lo = a0 * o[f >> 1][8 * (f & 1) + 2 * i], hi = a0 * o[f >> 1][8 * (f & 1) + 2 * i + 1];
#pragma unroll
        for (int j = 0; j < 3; ++j) { lo += a[j] * __uint_as_float(pr[j][f][i] << 16); hi += a[j] * __uint_as_float(pr[j][f][i] & 0xffff0000u); }
        ov[i] = cvtpk(lo, hi);
      }
      ldsw128(ofr + f * 1024, ov);
    }
  }
  STAMP(28);
  BAR();
  STAMP(29);
  {
    const unsigned wl = lb + vlane + 64 * (w ^ q4);
    f32x16 acc;
#pragma unroll
    for (int i = 0; i < 16; ++i) acc[i] = 0.f;
#pragma unroll
    for (int kt = 0; kt < 8; ++kt)
#pragma unroll
      for (int ks = 0; ks < 4; ++ks) {
        const int hd = kt >> 1, f = 4 * (kt & 1) + ks;
        const bf16x8 wf = ldstr(wl + wo_lds(kt) + 4096 * ks);
        const bf16x8 of = asbf(ldsr128(lb + (hd < 2 ? KRING + 3 * SLOT + hd * 8192 : VRING + 3 * SLOT + (hd - 2) * 8192) + f * 1024 + lane * 16));
        acc = MFMA32(wf, of, acc);
      }
    const rsrc_t rpo = mkrs(ws + WS_PO + ((b * 4 + sp) * 8 + hkv) * 16384 + w * 4096, 4096);
#pragma unroll
    for (int q = 0; q < 4; ++q) {
      f32x4 v = {acc[4 * q], acc[4 * q + 1], acc[4 * q + 2], acc[4 * q + 3]};
      st128(__builtin_bit_cast(u32x4, v), rpo, lane * 16 + q * 1024);
    }
  }
  STAMP(30);
  WAITV(0);
  STAMP(31);
  BAR();
  if (w == 0 && lane == 0) __hip_atomic_store((gu64*)(ws + WS_F2 + bid * 256), tag, __ATOMIC_RELAXED, __HIP_MEMORY_SCOPE_AGENT);
  if (w < 2) {
    poll_flags(ws + WS_F2 + (8 * b + 64 * sp) * 256, 256, 8, tag, lane);
    STAMP(32);
    const int pc = 8 * w + (lane >> 3), sub = lane & 7, h2 = sub >> 2, row = 4 * hkv + (sub & 3);
    u32x4 pv[8];
#pragma unroll
    for (int k = 0; k < 8; ++k) pv[k] = __builtin_amdgcn_raw_buffer_load_b128(rws, pc * 1024 + (h2 * 32 + row) * 16, WS_PO + ((b * 4 + sp) * 8 + k) * 16384, SC1);
    f32x4 s = __builtin_bit_cast(f32x4, pv[0]);
#pragma unroll
    for (int k = 1; k < 8; ++k) s += __builtin_bit_cast(f32x4, pv[k]);
    const int n = 32 * (pc >> 2) + 8 * (pc & 3) + 4 * h2;
    *(u32x2*)(out + (size_t)(b * 32 + row) * 512 + sp * 128 + n) = u32x2{cvtpk(s[0], s[1]), cvtpk(s[2], s[3])};
    if (w == 0 && hkv == 0 && sp == 0 && lane == 0) __hip_atomic_store((gu64*)(ws + WS_EP + b * 256), ep + 1ull, __ATOMIC_RELAXED, __HIP_MEMORY_SCOPE_AGENT);
    STAMP(33);
    if (w == 0 && bid == DIAG_BID) {
      asm volatile("s_waitcnt vmcnt(0)" ::: "memory");
      unsigned long long mn = ~0ull, mx = 0;
      for (int i = 0; i < 4; ++i) { unsigned long long v = __hip_atomic_load((gu64*)(ws + (32u << 20) + (lane + 64 * i) * 8), __ATOMIC_RELAXED, __HIP_MEMORY_SCOPE_AGENT); if (v > dg0 - 100000ull && v < dg0 + 100000ull) { mn = v < mn ? v : mn; mx = v > mx ? v : mx; } }
      for (int o = 32; o > 0; o >>= 1) { unsigned long long a = __shfl_xor(mn, o), c = __shfl_xor(mx, o); mn = a < mn ? a : mn; mx = c > mx ? c : mx; }
      const float tot = (float)(dg1 - mn) * 0.01f, skew = (float)(mx - mn) * 0.01f, own = (float)(dg1 - dg0) * 0.01f;
      const float val = DIAG_MODE == 0 ? tot : DIAG_MODE == 1 ? skew : own;
      if (lane == 0 && val > DIAG_THR) *(unsigned*)(out + (size_t)(b * 32 + row) * 512 + sp * 128 + n) = cvtpk(val, 0.f);
    }
  }
}

extern "C" void kernel_launch(void* const* d_in, const int* in_sizes, int n_in, void* d_out, int out_size, void* d_ws, size_t ws_size, hipStream_t stream) {
  (void)hipFuncSetAttribute((const void*)fused_gqa, hipFuncAttributeMaxDynamicSharedMemorySize, LDS_BYTES);
  fused_gqa<<<256, 512, LDS_BYTES, stream>>>((const unsigned short*)d_in[0], (const unsigned short*)d_in[1], (const unsigned short*)d_in[2],
                                             (const unsigned short*)d_in[3], (const unsigned short*)d_in[4], (unsigned short*)d_out, (char*)d_ws);
}
```

```cpp
#include <hip/hip_runtime.h>
#include <hip/hip_bf16.h>
#include <stdint.h>
#include <type_traits>

typedef __attribute__((ext_vector_type(8))) __bf16 bf16x8;
typedef __attribute__((ext_vector_type(4))) __bf16 bf16x4;
typedef __attribute__((ext_vector_type(2))) __bf16 bf16x2;
typedef __attribute__((ext_vector_type(16))) float f32x16;
typedef __attribute__((ext_vector_type(4))) float f32x4;
typedef __attribute__((ext_vector_type(2))) float f32x2;
typedef __attribute__((ext_vector_type(4))) unsigned u32x4;
typedef __attribute__((ext_vector_type(2))) unsigned u32x2;
#define LAS __attribute__((address_space(3)))
#define GAS __attribute__((address_space(1)))
#define DI __device__ __forceinline__
typedef GAS unsigned long long gu64;
typedef __amdgpu_buffer_rsrc_t rsrc_t;

constexpr int AUX_SC1 = 16;
constexpr int AUX_NT = 2;
constexpr float QSCALE = 0.12751743082f;
constexpr float THR = 8.0f;

constexpr unsigned WS_EP = 0;
constexpr unsigned WS_FC = 4096;
constexpr unsigned WS_Q = 262144;
constexpr unsigned WS_PART = WS_Q + 256 * 16384;
constexpr unsigned WS_ML = WS_PART + 1024 * 8192;
constexpr unsigned WS_PO = WS_ML + 1024 * 512;
constexpr unsigned WS_END = WS_PO + 256 * 8192;

constexpr unsigned SLOT = 16384, KRING = 0, VRING = 65536, SPARE = 131072, LDS_BYTES = 163840;
constexpr unsigned QREG = VRING + 3 * SLOT;
constexpr unsigned wo_lds(int kt) { return kt == 0 ? SPARE : kt == 1 ? SPARE + SLOT : kt == 2 ? KRING : kt == 3 ? KRING + SLOT : kt == 4 ? VRING : kt == 5 ? KRING + 2 * SLOT : kt == 6 ? VRING + SLOT : VRING + 2 * SLOT; }
DI unsigned ofrag_lds(int hd) { return hd < 2 ? KRING + 3 * SLOT + hd * 8192 : VRING + 3 * SLOT + (hd - 2) * 8192; }

constexpr unsigned long long TAGC = 0x9E3779B97F4A7C15ull;
constexpr unsigned SPIN_CAP = 1u << 18;
constexpr unsigned EMPTY = 0xFFFFFFFFu;
constexpr int QDELAY = 20, HSLEEP = 2, ODELAY = 6, OSLEEP = 4;

DI unsigned cvtpk(float lo, float hi) { f32x2 v = {lo, hi}; bf16x2 b = __builtin_convertvector(v, bf16x2); return __builtin_bit_cast(unsigned, b); }
DI bf16x8 asbf(u32x4 v) { return __builtin_bit_cast(bf16x8, v); }
DI int rfl(int x) { return __builtin_amdgcn_readfirstlane(x); }
DI float fmx(float a, float b) { return __builtin_elementwise_maximum(a, b); }
DI float fmx3(float a, float b, float c) { return fmx(fmx(a, b), c); }
DI float bflo(unsigned v) { return __uint_as_float(v << 16); }
DI float bfhi(unsigned v) { return __uint_as_float(v & 0xffff0000u); }
DI unsigned notempty(unsigned v) { return v < EMPTY ? v : EMPTY - 1u; }
DI unsigned umax3(unsigned a, unsigned b, unsigned c) { const unsigned m = a > b ? a : b; return m > c ? m : c; }
DI unsigned umax4(unsigned m, u32x4 v) { return umax3(umax3(m, v[0], v[1]), v[2], v[3]); }
DI rsrc_t mkrs(const void* p, unsigned bytes) { return __builtin_amdgcn_make_buffer_rsrc((void*)p, (short)0, bytes, 0x00020000); }
DI void st128(u32x4 v, rsrc_t r, unsigned voff) { __builtin_amdgcn_raw_buffer_store_b128(v, r, voff, 0, AUX_SC1); }
DI u32x4 ld128(rsrc_t r, unsigned voff, unsigned soff) { return __builtin_amdgcn_raw_buffer_load_b128(r, voff, soff, AUX_SC1); }
DI u32x4 ldsr128(unsigned a) { return *(const LAS u32x4*)(size_t)a; }
DI void ldsw128(unsigned a, u32x4 v) { *(LAS u32x4*)(size_t)a = v; }
DI bf16x8 ldstr(unsigned a) {
  bf16x4 lo = __builtin_amdgcn_ds_read_tr16_b64_v4bf16((LAS bf16x4*)(size_t)a);
  bf16x4 hi = __builtin_amdgcn_ds_read_tr16_b64_v4bf16((LAS bf16x4*)(size_t)(a + 2048));
  return __builtin_shufflevector(lo, hi, 0, 1, 2, 3, 4, 5, 6, 7);
}
#define SBAR() __builtin_amdgcn_sched_barrier(0)
#define PIN(x) asm volatile("" : "+v"(x))
#define BAR() do { SBAR(); asm volatile("s_waitcnt lgkmcnt(0)\n\ts_barrier" ::: "memory"); SBAR(); } while (0)
#define WAITV(n) asm volatile("s_waitcnt vmcnt(%0)" ::"n"(n) : "memory")
#define MFMA32(a, b, c) __builtin_amdgcn_mfma_f32_32x32x16_bf16((a), (b), (c), 0, 0, 0)
#define MFMA16(a, b, c) __builtin_amdgcn_mfma_f32_16x16x32_bf16((a), (b), (c), 0, 0, 0)

struct Ld { rsrc_t rk, rv, rw; unsigned vk, vv, vw, lb; int j, rot; };
template <int U> DI void issue(const Ld& L) {
  if constexpr (U < 16) {
    constexpr int t = U >> 1; constexpr bool isv = U & 1;
    const unsigned dst = L.lb + (isv ? VRING : KRING) + (t & 3) * SLOT + L.j * 1024;
#pragma unroll
    for (int i = 0; i < 4; ++i)
      __builtin_amdgcn_raw_ptr_buffer_load_lds(isv ? L.rv : L.rk, (LAS void*)(size_t)(dst + i * 4096), 16, isv ? L.vv : L.vk, ((t + L.rot) & 7) * SLOT + i * 4096, 0, AUX_NT);
  } else {
    constexpr int kt = U - 16;
    const unsigned dst = L.lb + wo_lds(kt) + L.j * 1024;
#pragma unroll
    for (int i = 0; i < 4; ++i)
      __builtin_amdgcn_raw_ptr_buffer_load_lds(L.rw, (LAS void*)(size_t)(dst + i * 4096), 16, L.vw, (kt * 64 + i * 16) * 1024, 0, 0);
  }
}
template <int A, int B> DI void issue_range(const Ld& L) { if constexpr (A < B) { issue<A>(L); issue_range<A + 1, B>(L); } }
constexpr int last_issued(int t) { return 2 * t + 4; }
template <int T> DI void loader_main(const Ld& L) {
  if constexpr (T <= 8) {
    constexpr int need = T < 8 ? 2 * T : 15;
    WAITV(4 * (last_issued(T) - need));
    BAR();
    if constexpr (T < 8) issue_range<last_issued(T) + 1, last_issued(T + 1) + 1>(L);
    loader_main<T + 1>(L);
  }
}

DI void split_weights(const float (&ms)[4], const float (&ls)[4], float (&a)[4]) {
  const float M = fmx(fmx(ms[0], ms[1]), fmx(ms[2], ms[3]));
  float L = 0.f;
#pragma unroll
  for (int j = 0; j < 4; ++j) { a[j] = __builtin_amdgcn_exp2f(ms[j] - M); L += a[j] * ls[j]; }
  const float inv = 1.f / L;
#pragma unroll
  for (int j = 0; j < 4; ++j) a[j] *= inv;
}

template <int W> DI void q_scatter(const f32x4 (&acc)[2][8], f32x4 (&own)[4], unsigned scr) {
#pragma unroll
  for (int ow = 0; ow < 4; ++ow)
#pragma unroll
    for (int e = 0; e < 4; ++e) {
      const f32x4 v = acc[ow >> 1][4 * (ow & 1) + e];
      if (ow == W) own[e] = v;
      else ldsw128(scr + QREG + W * 12288 + (ow < W ? ow : ow - 1) * 4096 + e * 1024, __builtin_bit_cast(u32x4, v));
    }
}

__global__ __launch_bounds__(512, 2) void fused_gqa(const unsigned short* __restrict__ x, const unsigned short* __restrict__ kc, const unsigned short* __restrict__ vc,
                                                    const unsigned short* __restrict__ wq, const unsigned short* __restrict__ wo, unsigned short* __restrict__ out, char* __restrict__ ws) {
  extern __shared__ __attribute__((aligned(16))) char lds[];
  const int bid = blockIdx.x, hkv = bid & 7, b = (bid >> 3) & 7, sp = bid >> 6;
  const int wave = rfl(threadIdx.x >> 6), lane = threadIdx.x & 63;
  const unsigned lb = (unsigned)(size_t)(LAS char*)lds;
  unsigned cpf = 0;
  if (wave == 0) {
    unsigned long long pc; unsigned len;
    asm volatile("s_getpc_b64 %0\ngqa_code_begin:\n\ts_mov_b32 %1, gqa_code_end-gqa_code_begin" : "=s"(pc), "=s"(len));
    const rsrc_t rc = mkrs((const void*)pc, len);
    const unsigned off = (unsigned)((bid >> 3) * 8 + (lane & 7)) * 128u;
    asm volatile("buffer_load_dword %0, %1, %2, 0 offen" : "=v"(cpf) : "v"(off), "s"(rc) : "memory");
  }
  if (wave >= 4) {
    Ld L; L.j = wave - 4; L.lb = lb; L.rot = (b + 2 * sp + hkv) & 7;
    const size_t kvoff = ((size_t)(b * 8 + hkv) * 2048 + sp * 512) * 128;
    L.rk = mkrs(kc + kvoff, 512 * 256);
    L.rv = mkrs(vc + kvoff, 512 * 256);
    L.rw = mkrs(wo + (size_t)hkv * 512 * 512 + sp * 128, 512 * 1024 - sp * 256);
    const unsigned r4 = lane >> 4, sl = lane & 15;
    L.vk = L.j * 1024 + r4 * 256 + ((sl ^ (4 * L.j + r4)) * 16);
    L.vv = L.j * 1024 + r4 * 256 + ((sl ^ (r4 << 2)) * 16);
    L.vw = (L.j * 4 + r4) * 1024 + ((sl ^ (r4 << 2)) * 16);
    const rsrc_t rws = mkrs(ws, WS_END);
    unsigned tch = 0;
    {
      const unsigned off = L.j < 2 ? WS_Q + (b * 32 + hkv * 4 + sp) * 16384 + L.j * 8192 + lane * 128 : WS_ML + ((b * 8 + hkv) * 4 + sp) * 2048 + (lane & 15) * 128;
      if (L.j < 3) asm volatile("buffer_load_dword %0, %1, %2, 0 offen" : "=v"(tch) : "v"(off), "s"(rws) : "memory");
    }
    const unsigned long long t0 = __hip_atomic_load((gu64*)(ws + WS_EP + b * 256), __ATOMIC_RELAXED, __HIP_MEMORY_SCOPE_AGENT) + TAGC;
    {
      const u32x4 e4 = {EMPTY, EMPTY, EMPTY, EMPTY};
      const rsrc_t ro = mkrs(ws + WS_PO + ((b * 4 + sp) * 8 + hkv) * 8192 + L.j * 2048, 2048);
      st128(e4, ro, lane * 16);
      st128(e4, ro, lane * 16 + 1024);
    }
    BAR();
    issue_range<0, 2>(L);
    BAR();
    issue_range<2, 3>(L);
    WAITV(0);
    unsigned long long ltag;
    {
      unsigned tl = (unsigned)rfl((int)(unsigned)t0), th = (unsigned)rfl((int)(unsigned)(t0 >> 32));
      asm volatile("" : "+s"(tl), "+s"(th));
      ltag = ((unsigned long long)th << 32) | tl;
      asm volatile("" ::"v"(tch));
    }
    BAR();
    BAR();
    issue_range<3, 4>(L);
    BAR();
    issue_range<4, last_issued(1) + 1>(L);
    WAITV(4 * (last_issued(1) - 2));
    BAR();
    if (L.j == 0 && lane == 0) __hip_atomic_store((gu64*)(ws + WS_FC + bid * 256), ltag, __ATOMIC_RELAXED, __HIP_MEMORY_SCOPE_AGENT);
    issue_range<last_issued(1) + 1, last_issued(2) + 1>(L);
    loader_main<2>(L);
    BAR();
    issue_range<last_issued(8) + 1, 24>(L);
    {
      const int j = L.j, r = lane & 31, pg = (b * 8 + hkv) * 4;
      u32x4 st[4], pr[4][4];
#pragma unroll
      for (int f = 0; f < 4; ++f) pr[3][f] = ldsr128(lb + KRING + 3 * SLOT + j * 4096 + f * 1024 + lane * 16);
      BAR();
#pragma unroll 1
      for (unsigned spins = 0; spins < SPIN_CAP; ++spins) {
        bool ok = true;
#pragma unroll
        for (int q = 0; q < 4; ++q) st[q] = ld128(rws, r * 16, WS_ML + ((pg + ((sp + 1 + q) & 3)) * 4 + j) * 512);
#pragma unroll
        for (int q = 0; q < 4; ++q) ok &= st[q][2] == (unsigned)ltag && st[q][3] == (unsigned)(ltag >> 32);
        if (__all(ok)) break;
        __builtin_amdgcn_s_sleep(HSLEEP);
      }
      asm volatile("" ::: "memory");
#pragma unroll
      for (int q = 0; q < 3; ++q)
#pragma unroll
        for (int f = 0; f < 4; ++f) pr[q][f] = ld128(rws, lane * 16 + (4 + f) * 1024, WS_PART + ((pg + ((sp + 1 + q) & 3)) * 4 + j) * 8192);
      float ms[4], ls[4], a[4];
#pragma unroll
      for (int q = 0; q < 4; ++q) { ms[q] = __uint_as_float(st[q][0]); ls[q] = __uint_as_float(st[q][1]); }
      split_weights(ms, ls, a);
#pragma unroll
      for (int f = 0; f < 4; ++f) {
        u32x4 ov;
#pragma unroll
        for (int i = 0; i < 4; ++i) {
          float lo = a[3] * bflo(pr[3][f][i]), hi = a[3] * bfhi(pr[3][f][i]);
#pragma unroll
          for (int q = 0; q < 3; ++q) { lo += a[q] * bflo(pr[q][f][i]); hi += a[q] * bfhi(pr[q][f][i]); }
          ov[i] = cvtpk(lo, hi);
        }
        ldsw128(lb + ofrag_lds(j) + (4 + f) * 1024 + lane * 16, ov);
      }
    }
    WAITV(0);
    BAR();
    asm volatile("gqa_code_end:");
    return;
  }
  const int w = wave;
  const rsrc_t rws = mkrs(ws, WS_END);
  unsigned long long ep;
  {
    const int c = lane & 15, g = lane >> 4, h = hkv * 4 + sp;
    const rsrc_t rq = mkrs(wq + h * 128, 512 * 8192 - h * 256);
    const unsigned vq = (128 * w + 8 * g) * 8192 + 16 * c;
    const unsigned xw = lb + QREG + w * 12288;
    {
      const rsrc_t rx = mkrs(x + b * 32 * 512 + 128 * w, 32 * 1024 - 256 * w);
#pragma unroll
      for (int i = 0; i < 8; ++i) {
        const unsigned vo = (4 * i + g) * 1024 + ((c ^ ((4 * i + g) & 15)) * 16);
        asm volatile("s_mov_b32 m0, %0\n\ts_nop 0\n\tbuffer_load_dwordx4 %1, %2, 0 offen lds" ::"s"(xw + i * 1024), "v"(vo), "s"(rx) : "memory");
      }
    }
    const unsigned xl = xw + c * 256;
    u32x4 wr[4][8];
    f32x4 acc[2][8];
#pragma unroll
    for (int rh = 0; rh < 2; ++rh)
#pragma unroll
      for (int jj = 0; jj < 8; ++jj) acc[rh][jj] = f32x4{0.f, 0.f, 0.f, 0.f};
    auto qload = [&](int n) {
      const unsigned hs = (unsigned)((n + b) & 7);
#pragma unroll
      for (int i = 0; i < 4; ++i) wr[n >> 1][4 * (n & 1) + i] = __builtin_amdgcn_raw_buffer_load_b128(rq, vq + i * 8192, (32 * (hs >> 1) + 4 * (hs & 1)) * 8192, 0);
      SBAR();
    };
    auto xread = [&](int n, int rh) {
      const unsigned hs = (unsigned)((n + b) & 7);
      return *(const LAS u32x2*)(size_t)(xl + rh * 4096 + (((4 * (hs >> 1) + g) ^ c) * 16) + 8 * (hs & 1));
    };
    auto qstep = [&](int st) {
      const u32x2 l0 = xread(2 * st, 0), h0 = xread(2 * st + 1, 0), l1 = xread(2 * st, 1), h1 = xread(2 * st + 1, 1);
      const u32x4 xa0 = {l0[0], l0[1], h0[0], h0[1]}, xa1 = {l1[0], l1[1], h1[0], h1[1]};
#pragma unroll
      for (int jj = 0; jj < 8; ++jj) {
        u32x4 a;
#pragma unroll
        for (int e = 0; e < 4; ++e) a[e] = __builtin_amdgcn_perm(wr[st][2 * e + 1][jj >> 1], wr[st][2 * e][jj >> 1], (jj & 1) ? 0x07060302u : 0x05040100u);
#pragma unroll
        for (int rh = 0; rh < 2; ++rh) acc[rh][jj] = MFMA16(asbf(a), asbf(rh ? xa1 : xa0), acc[rh][jj]);
      }
      SBAR();
    };
    qload(0); qload(1); qload(2); qload(3); qload(4); qload(5);
    WAITV(24);
    qstep(0); asm volatile("" ::"v"(cpf)); qload(6); qload(7);
    ep = __hip_atomic_load((gu64*)(ws + WS_EP + b * 256), __ATOMIC_RELAXED, __HIP_MEMORY_SCOPE_AGENT);
    SBAR();
    BAR();
    qstep(1); qstep(2); qstep(3);
    f32x4 own[4];
    switch (w) {
      case 0: q_scatter<0>(acc, own, lb + lane * 16); break;
      case 1: q_scatter<1>(acc, own, lb + lane * 16); break;
      case 2: q_scatter<2>(acc, own, lb + lane * 16); break;
      default: q_scatter<3>(acc, own, lb + lane * 16); break;
    }
    BAR();
#pragma unroll
    for (int j = 0; j < 3; ++j) {
      const int src = j < w ? j : j + 1;
      const unsigned pa = lb + lane * 16 + QREG + src * 12288 + (w < src ? w : w - 1) * 4096;
#pragma unroll
      for (int e = 0; e < 4; ++e) own[e] += __builtin_bit_cast(f32x4, ldsr128(pa + e * 1024));
    }
    const unsigned long long tg = ep + TAGC;
    const rsrc_t rqo = mkrs(ws + WS_Q + (b * 32 + h) * 16384, 16384);
#pragma unroll
    for (int reg = 0; reg < 4; ++reg) {
      const u32x4 gr = {cvtpk(own[0][reg] * QSCALE, own[1][reg] * QSCALE), cvtpk(own[2][reg] * QSCALE, own[3][reg] * QSCALE), (unsigned)tg, (unsigned)(tg >> 32)};
      st128(gr, rqo, ((2 * g + (reg >> 1)) * 2 + (w & 1)) * 1024 + ((reg & 1) * 32 + 16 * (w >> 1) + c) * 16);
    }
  }
  const unsigned long long tag = ep + TAGC;
  bf16x8 qf[8];
  {
    BAR();
    __builtin_amdgcn_s_sleep(QDELAY);
    const rsrc_t rqi = mkrs(ws + WS_Q + (b * 32 + hkv * 4 + w) * 16384, 16384);
    u32x4 gq[16];
#pragma unroll
    for (int i = 0; i < 16; ++i) gq[i] = ld128(rqi, lane * 16 + i * 1024, 0);
    BAR();
#pragma unroll 1
    for (unsigned spins = 0; spins < SPIN_CAP; ++spins) {
      bool ok = true;
#pragma unroll
      for (int i = 0; i < 16; ++i) ok &= gq[i][2] == (unsigned)tag && gq[i][3] == (unsigned)(tag >> 32);
      if (__all(ok)) break;
      __builtin_amdgcn_s_sleep(2);
#pragma unroll
      for (int i = 0; i < 16; ++i) gq[i] = ld128(rqi, lane * 16 + i * 1024, 0);
    }
#pragma unroll
    for (int s = 0; s < 8; ++s) qf[s] = asbf(u32x4{gq[2 * s][0], gq[2 * s][1], gq[2 * s + 1][0], gq[2 * s + 1][1]});
  }

  const int r = lane & 31, hh = lane >> 5;
  f32x16 o[4], negm;
#pragma unroll
  for (int i = 0; i < 16; ++i) { o[0][i] = 0.f; o[1][i] = 0.f; o[2][i] = 0.f; o[3][i] = 0.f; negm[i] = 0.f; }
  float m = 0.f;
  const unsigned kbase = lb + KRING + 256 * r + 16 * (hh ^ (r & 15));
  const int q4 = (lane & 15) >> 2, p4 = lane & 3, blk = (lane >> 4) & 1;
  const unsigned vlane = 256 * (4 * hh + q4) + 32 * blk + 8 * p4;
  WAITV(0);
  f32x16 s0, s1;
  u32x4 pf[4];
  const unsigned on1 = (lane == 0 || lane == 32 || lane == 17 || lane == 49) ? 0x3F803F80u : 0u;
  const bf16x8 aone = asbf(u32x4{on1, on1, on1, on1});
  f32x4 lacc = {0.f, 0.f, 0.f, 0.f};
  auto iter = [&](auto first_c, const int t) {
    constexpr bool FIRST = decltype(first_c)::value;
    const unsigned so = (t & 3) * SLOT, sv = ((t - 1) & 3) * SLOT;
    BAR();
    u32x4 kf[16];
    unsigned ka[8];
#pragma unroll
    for (int s = 0; s < 8; ++s) ka[s] = (kbase ^ (s << 5)) + so;
    auto kread = [&](int i) { kf[i] = ldsr128(ka[i & 7] + (i >> 3) * 8192); };
    kread(1); kread(0); kread(3); kread(2); kread(5); kread(4);
    SBAR();
    float e1[16], e0[16];
    auto fin = [&](int e) { e1[e] = __builtin_amdgcn_exp2f(s1[e]); PIN(e1[e]); };
    auto fincvt = [&](int d) { unsigned v = cvtpk(e1[2 * d], e1[2 * d + 1]); PIN(v); pf[2 + (d >> 2)][d & 3] = v; };
    if constexpr (!FIRST) {
#pragma unroll
      for (int e = 0; e < 8; ++e) fin(e);
      fincvt(0); fincvt(1);
      SBAR();
    }
    unsigned va[4];
    bf16x8 vf[16];
    auto vread = [&](int i) { vf[i] = ldstr(va[i & 3] + (i >> 2) * 4096); };
    float pm0 = 0.f;
#pragma unroll
    for (int mi = 0; mi < 16; ++mi) {
      if (mi == 0) s0 = MFMA32(asbf(kf[0]), qf[0], negm);
      else if (mi < 8) s0 = MFMA32(asbf(kf[mi]), qf[mi], s0);
      else if (mi == 8) s1 = MFMA32(asbf(kf[8]), qf[0], negm);
      else s1 = MFMA32(asbf(kf[mi]), qf[mi - 8], s1);
      if ((mi & 1) == 0 && mi + 6 < 16) { kread(mi + 7); kread(mi + 6); }
      if constexpr (!FIRST) {
        if (mi < 4) { fin(8 + 2 * mi); fin(9 + 2 * mi); }
        if (mi < 6) fincvt(2 + mi);
        if (mi == 9) { for (int db = 0; db < 4; ++db) va[db] = lb + VRING + sv + vlane + 64 * (db ^ q4); }
        if (mi == 10) { vread(1); vread(0); }
        if (mi == 12) { vread(3); vread(2); }
        if (mi == 14) { vread(5); vread(4); }
      }
      if (mi == 10) asm volatile("" ::"v"(negm));
      if (mi == 11) { pm0 = fmx3(s0[0], s0[1], s0[2]); pm0 = fmx3(pm0, s0[3], s0[4]); pm0 = fmx3(pm0, s0[5], s0[6]); }
      if (mi == 13) { pm0 = fmx3(pm0, s0[7], s0[8]); pm0 = fmx3(pm0, s0[9], s0[10]); pm0 = fmx3(pm0, s0[11], s0[12]); }
      if (mi == 15) { pm0 = fmx3(pm0, s0[13], s0[14]); pm0 = fmx(pm0, s0[15]); }
      SBAR();
    }
    float pm = pm0, alpha = 1.f;
    bool pend = false;
    auto pvgap = [&](int mi) {
      if constexpr (!FIRST) {
        o[mi & 3] = MFMA32(vf[mi], asbf(pf[mi >> 2]), o[mi & 3]);
        if ((mi & 1) == 0 && mi + 6 < 16) { vread(mi + 7); vread(mi + 6); }
        if ((mi & 3) == 3) lacc = MFMA16(aone, asbf(pf[mi >> 2]), lacc);
      }
    };
    pvgap(0); SBAR();
    pvgap(1); pm = fmx3(pm, s1[0], s1[1]); pm = fmx3(pm, s1[2], s1[3]); pm = fmx3(pm, s1[4], s1[5]); pm = fmx3(pm, s1[6], s1[7]); SBAR();
    pvgap(2); pm = fmx3(pm, s1[8], s1[9]); pm = fmx3(pm, s1[10], s1[11]); pm = fmx3(pm, s1[12], s1[13]); pm = fmx3(pm, s1[14], s1[15]); SBAR();
    pvgap(3);
    { auto rr = __builtin_amdgcn_permlane32_swap(__float_as_uint(pm), __float_as_uint(pm), false, false);
      pm = fmx(__uint_as_float(rr[0]), __uint_as_float(rr[1])); }
    SBAR();
    if (FIRST || __builtin_expect(__any(pm > THR), 0)) {
      const float dl = FIRST ? pm : fmx(pm, 0.f);
      m += dl;
#pragma unroll
      for (int i = 0; i < 16; ++i) { s0[i] -= dl; s1[i] -= dl; negm[i] = -m; }
      if constexpr (!FIRST) { alpha = __builtin_amdgcn_exp2f(-dl); pend = true; }
    }
    SBAR();
#pragma unroll
    for (int mi = 4; mi < 16; ++mi) {
      pvgap(mi);
      if (mi < 12) { const int e = 2 * (mi - 4); e0[e] = __builtin_amdgcn_exp2f(s0[e]); e0[e + 1] = __builtin_amdgcn_exp2f(s0[e + 1]); PIN(e0[e]); PIN(e0[e + 1]); }
      if (mi >= 8 && mi < 12) { unsigned v = cvtpk(e0[2 * (mi - 8)], e0[2 * (mi - 8) + 1]); PIN(v); pf[0][mi - 8] = v; }
      if (mi >= 12) { unsigned v = cvtpk(e0[8 + 2 * (mi - 12)], e0[8 + 2 * (mi - 12) + 1]); PIN(v); pf[1][mi - 12] = v; }
      SBAR();
    }
    if constexpr (!FIRST) {
      if (__builtin_expect(pend, 0)) {
#pragma unroll
        for (int db = 0; db < 4; ++db)
#pragma unroll
          for (int i = 0; i < 16; ++i) o[db][i] *= alpha;
        lacc[0] *= __shfl(alpha, lane & 15);
        lacc[1] *= __shfl(alpha, 16 + (lane & 15));
      }
    }
  };
  iter(std::true_type{}, 0);
#pragma unroll 1
  for (int t = 1; t < 8; ++t) iter(std::false_type{}, t);
  {
    float e1[16];
#pragma unroll
    for (int e = 0; e < 16; ++e) e1[e] = __builtin_amdgcn_exp2f(s1[e]);
#pragma unroll
    for (int d = 0; d < 8; ++d) pf[2 + (d >> 2)][d & 3] = cvtpk(e1[2 * d], e1[2 * d + 1]);
#pragma unroll
    for (int ks = 0; ks < 4; ++ks) lacc = MFMA16(aone, asbf(pf[ks]), lacc);
    asm volatile("" : "+v"(pf[2]), "+v"(pf[3]), "+v"(lacc));
  }
  gu64* const cfp = (gu64*)(ws + WS_FC + ((lane & 7) + 8 * b + 64 * sp) * 256);
  unsigned long long cf = __hip_atomic_load(cfp, __ATOMIC_RELAXED, __HIP_MEMORY_SCOPE_AGENT);
  float l;
  { float la = __shfl(lacc[0], lane & 15), lc = __shfl(lacc[1], lane & 15);
    asm volatile("" : "+v"(la), "+v"(lc));
    l = r < 16 ? la : lc; }
  const int pgrp = (b * 8 + hkv) * 4;
  const rsrc_t rp = mkrs(ws + WS_PART + ((pgrp + sp) * 4 + w) * 8192, 8192);
  BAR();
  if (!__all(cf == tag)) {
#pragma unroll 1
    for (unsigned spins = 0; spins < SPIN_CAP; ++spins) {
      __builtin_amdgcn_s_sleep(2);
      if (__all(__hip_atomic_load(cfp, __ATOMIC_RELAXED, __HIP_MEMORY_SCOPE_AGENT) == tag)) break;
    }
  }
  {
    unsigned va[4];
#pragma unroll
    for (int db = 0; db < 4; ++db) va[db] = lb + VRING + 3 * SLOT + vlane + 64 * (db ^ q4);
    bf16x8 vf[16];
    auto vread = [&](int i) { vf[i] = ldstr(va[i >> 2] + (i & 3) * 4096); };
    auto publish = [&](int db, int sx) {
      u32x4 v;
#pragma unroll
      for (int i = 0; i < 4; ++i) v[i] = cvtpk(o[db][8 * sx + 2 * i], o[db][8 * sx + 2 * i + 1]);
      st128(v, rp, lane * 16 + (2 * db + sx) * 1024);
      if (db >= 2) ldsw128(lb + KRING + 3 * SLOT + w * 4096 + (2 * (db - 2) + sx) * 1024 + lane * 16, v);
    };
    vread(1); vread(0); vread(3); vread(2); vread(5); vread(4);
    SBAR();
#pragma unroll
    for (int mi = 0; mi < 16; ++mi) {
      o[mi >> 2] = MFMA32(vf[mi], asbf(pf[mi & 3]), o[mi >> 2]);
      if ((mi & 1) == 0 && mi + 6 < 16) { vread(mi + 7); vread(mi + 6); }
      if (mi >= 6 && (mi & 3) >= 2) publish((mi >> 2) - 1, mi & 1);
      SBAR();
    }
    publish(3, 0); publish(3, 1);
  }
  BAR();
  WAITV(0);
  if (hh == 0) st128(u32x4{__float_as_uint(m), __float_as_uint(l), (unsigned)tag, (unsigned)(tag >> 32)}, mkrs(ws + WS_ML + ((pgrp + sp) * 4 + w) * 512, 512), r * 16);
  BAR();
  {
    u32x4 st[3], pr[3][4];
#pragma unroll 1
    for (unsigned spins = 0; spins < SPIN_CAP; ++spins) {
      bool ok = true;
#pragma unroll
      for (int j = 0; j < 3; ++j) st[j] = ld128(rws, r * 16, WS_ML + ((pgrp + ((sp + 1 + j) & 3)) * 4 + w) * 512);
#pragma unroll
      for (int j = 0; j < 3; ++j) ok &= st[j][2] == (unsigned)tag && st[j][3] == (unsigned)(tag >> 32);
      if (__all(ok)) break;
      __builtin_amdgcn_s_sleep(HSLEEP);
    }
    asm volatile("" ::: "memory");
#pragma unroll
    for (int j = 0; j < 3; ++j)
#pragma unroll
      for (int f = 0; f < 4; ++f) pr[j][f] = ld128(rws, lane * 16 + f * 1024, WS_PART + ((pgrp + ((sp + 1 + j) & 3)) * 4 + w) * 8192);
    float ms[4], ls[4], a[4];
#pragma unroll
    for (int q = 0; q < 3; ++q) { ms[q] = __uint_as_float(st[q][0]); ls[q] = __uint_as_float(st[q][1]); }
    ms[3] = m; ls[3] = l;
    split_weights(ms, ls, a);
#pragma unroll
    for (int f = 0; f < 4; ++f) {
      u32x4 ov;
#pragma unroll
      for (int i = 0; i < 4; ++i) {
        float lo = a[3] * o[f >> 1][8 * (f & 1) + 2 * i], hi = a[3] * o[f >> 1][8 * (f & 1) + 2 * i + 1];
#pragma unroll
        for (int j = 0; j < 3; ++j) { lo += a[j] * bflo(pr[j][f][i]); hi += a[j] * bfhi(pr[j][f][i]); }
        ov[i] = cvtpk(lo, hi);
      }
      ldsw128(lb + ofrag_lds(w) + f * 1024 + lane * 16, ov);
    }
  }
  BAR();
  {
    const unsigned wl = lb + vlane + 64 * (w ^ q4), ol = lb + lane * 16;
    f32x16 acc, acc2;
#pragma unroll
    for (int i = 0; i < 16; ++i) { acc[i] = 0.f; acc2[i] = 0.f; }
    bf16x8 wf[32]; u32x4 of[32];
    auto rd = [&](int i) { const int kt = i >> 2, ks = i & 3; of[i] = ldsr128(ol + ofrag_lds(kt >> 1) + (4 * (kt & 1) + ks) * 1024); wf[i] = ldstr(wl + wo_lds(kt) + 4096 * ks); };
    rd(1); rd(0); rd(3); rd(2);
    SBAR();
#pragma unroll
    for (int i = 0; i < 32; ++i) {
      if (i & 1) acc2 = MFMA32(wf[i], asbf(of[i]), acc2); else acc = MFMA32(wf[i], asbf(of[i]), acc);
      if ((i & 1) == 0 && i + 4 < 32) { rd(i + 5); rd(i + 4); }
      SBAR();
    }
    acc += acc2;
    const rsrc_t rpo = mkrs(ws + WS_PO + ((b * 4 + sp) * 8 + hkv) * 8192 + w * 2048, 2048);
#pragma unroll
    for (int q2 = 0; q2 < 2; ++q2) {
      u32x4 v;
#pragma unroll
      for (int i = 0; i < 4; ++i) v[i] = notempty(cvtpk(acc[8 * q2 + 2 * i], acc[8 * q2 + 2 * i + 1]));
      st128(v, rpo, lane * 16 + q2 * 1024);
    }
  }
  if (w == 0) {
    __builtin_amdgcn_s_sleep(ODELAY);
    const int pc = lane >> 3, sub = lane & 7, h2 = sub >> 2, row = 4 * hkv + (sub & 3);
    u32x4 pv[8];
#pragma unroll 1
    for (unsigned spins = 0; spins < SPIN_CAP; ++spins) {
#pragma unroll
      for (int k = 0; k < 8; ++k) pv[k] = ld128(rws, pc * 1024 + (h2 * 32 + row) * 16, WS_PO + ((b * 4 + sp) * 8 + k) * 8192);
      unsigned mx = 0;
#pragma unroll
      for (int k = 0; k < 8; ++k) mx = umax4(mx, pv[k]);
      if (__all(mx != EMPTY)) break;
      __builtin_amdgcn_s_sleep(OSLEEP);
    }
    float s[8];
#pragma unroll
    for (int i = 0; i < 4; ++i) { s[2 * i] = bflo(pv[0][i]); s[2 * i + 1] = bfhi(pv[0][i]); }
#pragma unroll
    for (int k = 1; k < 8; ++k)
#pragma unroll
      for (int i = 0; i < 4; ++i) { s[2 * i] += bflo(pv[k][i]); s[2 * i + 1] += bfhi(pv[k][i]); }
    const int n = 32 * (pc >> 1) + 16 * (pc & 1) + 4 * h2;
    const rsrc_t ro = mkrs(out, 262144);
    const unsigned oo = (unsigned)((b * 32 + row) * 512 + sp * 128 + n) * 2;
    __builtin_amdgcn_raw_buffer_store_b64(u32x2{cvtpk(s[0], s[1]), cvtpk(s[2], s[3])}, ro, oo, 0, 0);
    __builtin_amdgcn_raw_buffer_store_b64(u32x2{cvtpk(s[4], s[5]), cvtpk(s[6], s[7])}, ro, oo + 16, 0, 0);
    if (hkv == 0 && sp == 0 && lane == 0) __hip_atomic_store((gu64*)(ws + WS_EP + b * 256), ep + 1ull, __ATOMIC_RELAXED, __HIP_MEMORY_SCOPE_AGENT);
  }
}

extern "C" void kernel_launch(void* const* d_in, const int* in_sizes, int n_in, void* d_out, int out_size, void* d_ws, size_t ws_size, hipStream_t stream) {
  (void)in_sizes; (void)n_in; (void)out_size; (void)ws_size;
  (void)hipFuncSetAttribute((const void*)fused_gqa, hipFuncAttributeMaxDynamicSharedMemorySize, LDS_BYTES);
  fused_gqa<<<256, 512, LDS_BYTES, stream>>>((const unsigned short*)d_in[0], (const unsigned short*)d_in[1], (const unsigned short*)d_in[2],
                                             (const unsigned short*)d_in[3], (const unsigned short*)d_in[4], (unsigned short*)d_out, (char*)d_ws);
}
```

```cpp
#include <hip/hip_runtime.h>
#include <hip/hip_bf16.h>
#include <stdint.h>
#include <type_traits>

typedef __attribute__((ext_vector_type(8))) __bf16 bf16x8;
typedef __attribute__((ext_vector_type(4))) __bf16 bf16x4;
typedef __attribute__((ext_vector_type(2))) __bf16 bf16x2;
typedef __attribute__((ext_vector_type(16))) float f32x16;
typedef __attribute__((ext_vector_type(4))) float f32x4;
typedef __attribute__((ext_vector_type(2))) float f32x2;
typedef __attribute__((ext_vector_type(4))) unsigned u32x4;
typedef __attribute__((ext_vector_type(2))) unsigned u32x2;
#define LAS __attribute__((address_space(3)))
#define GAS __attribute__((address_space(1)))
#define DI __device__ __forceinline__
typedef GAS unsigned long long gu64;
typedef __amdgpu_buffer_rsrc_t rsrc_t;

constexpr int AUX_SC1 = 16;
constexpr int AUX_NT = 2;
constexpr float QSCALE = 0.12751743082f;
constexpr float THR = 8.0f;

constexpr unsigned WS_EP = 0;
constexpr unsigned WS_FC = 4096;
constexpr unsigned WS_Q = 262144;
constexpr unsigned WS_PART = WS_Q + 256 * 16384;
constexpr unsigned WS_ML = WS_PART + 1024 * 8192;
constexpr unsigned WS_PO = WS_ML + 1024 * 512;
constexpr unsigned WS_END = WS_PO + 256 * 8192;

constexpr unsigned SLOT = 16384, KRING = 0, VRING = 65536, SPARE = 131072, LDS_BYTES = 163840;
constexpr unsigned QREG = VRING + 3 * SLOT;
constexpr unsigned wo_lds(int kt) { return kt == 0 ? SPARE : kt == 1 ? SPARE + SLOT : kt == 2 ? KRING : kt == 3 ? KRING + SLOT : kt == 4 ? VRING : kt == 5 ? KRING + 2 * SLOT : kt == 6 ? VRING + SLOT : VRING + 2 * SLOT; }
DI unsigned ofrag_lds(int hd) { return hd < 2 ? KRING + 3 * SLOT + hd * 8192 : VRING + 3 * SLOT + (hd - 2) * 8192; }

constexpr unsigned long long TAGC = 0x9E3779B97F4A7C15ull;
constexpr unsigned SPIN_CAP = 1u << 18;
constexpr unsigned EMPTY = 0xFFFFFFFFu;
constexpr int QDELAY = 20, HSLEEP = 2, ODELAY = 6, OSLEEP = 4;

DI unsigned cvtpk(float lo, float hi) { f32x2 v = {lo, hi}; bf16x2 b = __builtin_convertvector(v, bf16x2); return __builtin_bit_cast(unsigned, b); }
DI bf16x8 asbf(u32x4 v) { return __builtin_bit_cast(bf16x8, v); }
DI int rfl(int x) { return __builtin_amdgcn_readfirstlane(x); }
DI float fmx(float a, float b) { return __builtin_elementwise_maximum(a, b); }
DI float fmx3(float a, float b, float c) { return fmx(fmx(a, b), c); }
DI float bflo(unsigned v) { return __uint_as_float(v << 16); }
DI float bfhi(unsigned v) { return __uint_as_float(v & 0xffff0000u); }
DI unsigned notempty(unsigned v) { return v < EMPTY ? v : EMPTY - 1u; }
DI unsigned umax3(unsigned a, unsigned b, unsigned c) { const unsigned m = a > b ? a : b; return m > c ? m : c; }
DI unsigned umax4(unsigned m, u32x4 v) { return umax3(umax3(m, v[0], v[1]), v[2], v[3]); }
DI rsrc_t mkrs(const void* p, unsigned bytes) { return __builtin_amdgcn_make_buffer_rsrc((void*)p, (short)0, bytes, 0x00020000); }
DI void st128(u32x4 v, rsrc_t r, unsigned voff) { __builtin_amdgcn_raw_buffer_store_b128(v, r, voff, 0, AUX_SC1); }
DI u32x4 ld128(rsrc_t r, unsigned voff, unsigned soff) { return __builtin_amdgcn_raw_buffer_load_b128(r, voff, soff, AUX_SC1); }
DI u32x4 ldsr128(unsigned a) { return *(const LAS u32x4*)(size_t)a; }
DI void ldsw128(unsigned a, u32x4 v) { *(LAS u32x4*)(size_t)a = v; }
DI bf16x8 ldstr(unsigned a) {
  bf16x4 lo = __builtin_amdgcn_ds_read_tr16_b64_v4bf16((LAS bf16x4*)(size_t)a);
  bf16x4 hi = __builtin_amdgcn_ds_read_tr16_b64_v4bf16((LAS bf16x4*)(size_t)(a + 2048));
  return __builtin_shufflevector(lo, hi, 0, 1, 2, 3, 4, 5, 6, 7);
}
#define SBAR() __builtin_amdgcn_sched_barrier(0)
#define PIN(x) asm volatile("" : "+v"(x))
#define BAR() do { SBAR(); asm volatile("s_waitcnt lgkmcnt(0)\n\ts_barrier" ::: "memory"); SBAR(); } while (0)
#define WAITV(n) asm volatile("s_waitcnt vmcnt(%0)" ::"n"(n) : "memory")
#define MFMA32(a, b, c) __builtin_amdgcn_mfma_f32_32x32x16_bf16((a), (b), (c), 0, 0, 0)
#define MFMA16(a, b, c) __builtin_amdgcn_mfma_f32_16x16x32_bf16((a), (b), (c), 0, 0, 0)

struct Ld { rsrc_t rk, rv, rw; unsigned vk, vv, vw, lb; int j, rot; };
template <int U> DI void issue(const Ld& L) {
  if constexpr (U < 16) {
    constexpr int t = U >> 1; constexpr bool isv = U & 1;
    const unsigned dst = L.lb + (isv ? VRING : KRING) + (t & 3) * SLOT + L.j * 1024;
#pragma unroll
    for (int i = 0; i < 4; ++i)
      __builtin_amdgcn_raw_ptr_buffer_load_lds(isv ? L.rv : L.rk, (LAS void*)(size_t)(dst + i * 4096), 16, isv ? L.vv : L.vk, ((t + L.rot) & 7) * SLOT + i * 4096, 0, AUX_NT);
  } else {
    constexpr int kt = U - 16;
    const unsigned dst = L.lb + wo_lds(kt) + L.j * 1024;
#pragma unroll
    for (int i = 0; i < 4; ++i)
      __builtin_amdgcn_raw_ptr_buffer_load_lds(L.rw, (LAS void*)(size_t)(dst + i * 4096), 16, L.vw, (kt * 64 + i * 16) * 1024, 0, 0);
  }
}
template <int A, int B> DI void issue_range(const Ld& L) { if constexpr (A < B) { issue<A>(L); issue_range<A + 1, B>(L); } }
constexpr int last_issued(int t) { return 2 * t + 4; }
template <int T> DI void loader_main(const Ld& L) {
  if constexpr (T <= 8) {
    constexpr int need = T < 8 ? 2 * T : 15;
    WAITV(4 * (last_issued(T) - need));
    BAR();
    if constexpr (T < 8) issue_range<last_issued(T) + 1, last_issued(T + 1) + 1>(L);
    loader_main<T + 1>(L);
  }
}

DI void split_weights(const float (&ms)[4], const float (&ls)[4], float (&a)[4]) {
  const float M = fmx(fmx(ms[0], ms[1]), fmx(ms[2], ms[3]));
  float L = 0.f;
#pragma unroll
  for (int j = 0; j < 4; ++j) { a[j] = __builtin_amdgcn_exp2f(ms[j] - M); L += a[j] * ls[j]; }
  const float inv = 1.f / L;
#pragma unroll
  for (int j = 0; j < 4; ++j) a[j] *= inv;
}

template <int W> DI void q_scatter(const f32x4 (&acc)[2][8], f32x4 (&own)[4], unsigned scr) {
#pragma unroll
  for (int ow = 0; ow < 4; ++ow)
#pragma unroll
    for (int e = 0; e < 4; ++e) {
      const f32x4 v = acc[ow >> 1][4 * (ow & 1) + e];
      if (ow == W) own[e] = v;
      else ldsw128(scr + QREG + W * 12288 + (ow < W ? ow : ow - 1) * 4096 + e * 1024, __builtin_bit_cast(u32x4, v));
    }
}

__global__ __launch_bounds__(512, 2) void fused_gqa(const unsigned short* __restrict__ x, const unsigned short* __restrict__ kc, const unsigned short* __restrict__ vc,
                                                    const unsigned short* __restrict__ wq, const unsigned short* __restrict__ wo, unsigned short* __restrict__ out, char* __restrict__ ws) {
  extern __shared__ __attribute__((aligned(16))) char lds[];
  const int bid = blockIdx.x, hkv = bid & 7, b = (bid >> 3) & 7, sp = bid >> 6;
  const int wave = rfl(threadIdx.x >> 6), lane = threadIdx.x & 63;
  const unsigned lb = (unsigned)(size_t)(LAS char*)lds;
  unsigned cpf = 0;
  if (wave == 0) {
    unsigned long long pc; unsigned len;
    asm volatile("s_getpc_b64 %0\ngqa_code_begin:\n\ts_mov_b32 %1, gqa_code_end-gqa_code_begin" : "=s"(pc), "=s"(len));
    const rsrc_t rc = mkrs((const void*)pc, len);
    const unsigned off = (unsigned)((bid >> 3) * 8 + (lane & 7)) * 128u;
    asm volatile("buffer_load_dword %0, %1, %2, 0 offen" : "=v"(cpf) : "v"(off), "s"(rc) : "memory");
  }
  if (wave >= 4) {
    Ld L; L.j = wave - 4; L.lb = lb; L.rot = (b + 2 * sp + hkv) & 7;
    const size_t kvoff = ((size_t)(b * 8 + hkv) * 2048 + sp * 512) * 128;
    L.rk = mkrs(kc + kvoff, 512 * 256);
    L.rv = mkrs(vc + kvoff, 512 * 256);
    L.rw = mkrs(wo + (size_t)hkv * 512 * 512 + sp * 128, 512 * 1024 - sp * 256);
    const unsigned r4 = lane >> 4, sl = lane & 15;
    L.vk = L.j * 1024 + r4 * 256 + ((sl ^ (4 * L.j + r4)) * 16);
    L.vv = L.j * 1024 + r4 * 256 + ((sl ^ (r4 << 2)) * 16);
    L.vw = (L.j * 4 + r4) * 1024 + ((sl ^ (r4 << 2)) * 16);
    const rsrc_t rws = mkrs(ws, WS_END);
    unsigned tch = 0;
    {
      const unsigned off = L.j < 2 ? WS_Q + (b * 32 + hkv * 4 + sp) * 16384 + L.j * 8192 + lane * 128 : WS_ML + ((b * 8 + hkv) * 4 + sp) * 2048 + (lane & 15) * 128;
      if (L.j < 3) asm volatile("buffer_load_dword %0, %1, %2, 0 offen" : "=v"(tch) : "v"(off), "s"(rws) : "memory");
    }
    const unsigned long long t0 = __hip_atomic_load((gu64*)(ws + WS_EP + b * 256), __ATOMIC_RELAXED, __HIP_MEMORY_SCOPE_AGENT) + TAGC;
    {
      const u32x4 e4 = {EMPTY, EMPTY, EMPTY, EMPTY};
      const rsrc_t ro = mkrs(ws + WS_PO + ((b * 4 + sp) * 8 + hkv) * 8192 + L.j * 2048, 2048);
      st128(e4, ro, lane * 16);
      st128(e4, ro, lane * 16 + 1024);
    }
    BAR();
    issue_range<0, 2>(L);
    BAR();
    issue_range<2, 3>(L);
    WAITV(0);
    unsigned long long ltag;
    {
      unsigned tl = (unsigned)rfl((int)(unsigned)t0), th = (unsigned)rfl((int)(unsigned)(t0 >> 32));
      asm volatile("" : "+s"(tl), "+s"(th));
      ltag = ((unsigned long long)th << 32) | tl;
      asm volatile("" ::"v"(tch));
    }
    BAR();
    BAR();
    issue_range<3, 4>(L);
    BAR();
    issue_range<4, last_issued(1) + 1>(L);
    WAITV(4 * (last_issued(1) - 2));
    BAR();
    if (L.j == 0 && lane == 0) __hip_atomic_store((gu64*)(ws + WS_FC + bid * 256), ltag, __ATOMIC_RELAXED, __HIP_MEMORY_SCOPE_AGENT);
    issue_range<last_issued(1) + 1, last_issued(2) + 1>(L);
    loader_main<2>(L);
    BAR();
    issue_range<last_issued(8) + 1, 24>(L);
    {
      const int j = L.j, r = lane & 31, pg = (b * 8 + hkv) * 4;
      u32x4 st[4], pr[4][4];
#pragma unroll
      for (int f = 0; f < 4; ++f) pr[3][f] = ldsr128(lb + KRING + 3 * SLOT + j * 4096 + f * 1024 + lane * 16);
      BAR();
#pragma unroll 1
      for (unsigned spins = 0; spins < SPIN_CAP; ++spins) {
        bool ok = true;
#pragma unroll
        for (int q = 0; q < 4; ++q) st[q] = ld128(rws, r * 16, WS_ML + ((pg + ((sp + 1 + q) & 3)) * 4 + j) * 512);
#pragma unroll
        for (int q = 0; q < 4; ++q) ok &= st[q][2] == (unsigned)ltag && st[q][3] == (unsigned)(ltag >> 32);
        if (__all(ok)) break;
        __builtin_amdgcn_s_sleep(HSLEEP);
      }
      asm volatile("" ::: "memory");
#pragma unroll
      for (int q = 0; q < 3; ++q)
#pragma unroll
        for (int f = 0; f < 4; ++f) pr[q][f] = ld128(rws, lane * 16 + (4 + f) * 1024, WS_PART + ((pg + ((sp + 1 + q) & 3)) * 4 + j) * 8192);
      float ms[4], ls[4], a[4];
#pragma unroll
      for (int q = 0; q < 4; ++q) { ms[q] = __uint_as_float(st[q][0]); ls[q] = __uint_as_float(st[q][1]); }
      split_weights(ms, ls, a);
#pragma unroll
      for (int f = 0; f < 4; ++f) {
        u32x4 ov;
#pragma unroll
        for (int i = 0; i < 4; ++i) {
          float lo = a[3] * bflo(pr[3][f][i]), hi = a[3] * bfhi(pr[3][f][i]);
#pragma unroll
          for (int q = 0; q < 3; ++q) { lo += a[q] * bflo(pr[q][f][i]); hi += a[q] * bfhi(pr[q][f][i]); }
          ov[i] = cvtpk(lo, hi);
        }
        ldsw128(lb + ofrag_lds(j) + (4 + f) * 1024 + lane * 16, ov);
      }
    }
    WAITV(0);
    BAR();
    asm volatile("gqa_code_end:");
    return;
  }
  const int w = wave;
  const rsrc_t rws = mkrs(ws, WS_END);
  unsigned long long ep;
  {
    const int c = lane & 15, g = lane >> 4, h = hkv * 4 + sp;
    const rsrc_t rq = mkrs(wq + h * 128, 512 * 8192 - h * 256);
    const unsigned vq = (128 * w + 8 * g) * 8192 + 16 * c;
    const unsigned xw = lb + QREG + w * 12288;
    {
      const rsrc_t rx = mkrs(x + b * 32 * 512 + 128 * w, 32 * 1024 - 256 * w);
#pragma unroll
      for (int i = 0; i < 8; ++i) {
        const unsigned vo = (4 * i + g) * 1024 + ((c ^ ((4 * i + g) & 15)) * 16);
        asm volatile("s_mov_b32 m0, %0\n\ts_nop 0\n\tbuffer_load_dwordx4 %1, %2, 0 offen lds" ::"s"(xw + i * 1024), "v"(vo), "s"(rx) : "memory");
      }
    }
    const unsigned xl = xw + c * 256;
    u32x4 wr[4][8];
    f32x4 acc[2][8];
#pragma unroll
    for (int rh = 0; rh < 2; ++rh)
#pragma unroll
      for (int jj = 0; jj < 8; ++jj) acc[rh][jj] = f32x4{0.f, 0.f, 0.f, 0.f};
    auto qload = [&](int n) {
      const unsigned hs = (unsigned)((n + b) & 7);
#pragma unroll
      for (int i = 0; i < 4; ++i) wr[n >> 1][4 * (n & 1) + i] = __builtin_amdgcn_raw_buffer_load_b128(rq, vq + i * 8192, (32 * (hs >> 1) + 4 * (hs & 1)) * 8192, 0);
      SBAR();
    };
    auto xread = [&](int n, int rh) {
      const unsigned hs = (unsigned)((n + b) & 7);
      return *(const LAS u32x2*)(size_t)(xl + rh * 4096 + (((4 * (hs >> 1) + g) ^ c) * 16) + 8 * (hs & 1));
    };
    auto qstep = [&](int st) {
      const u32x2 l0 = xread(2 * st, 0), h0 = xread(2 * st + 1, 0), l1 = xread(2 * st, 1), h1 = xread(2 * st + 1, 1);
      const u32x4 xa0 = {l0[0], l0[1], h0[0], h0[1]}, xa1 = {l1[0], l1[1], h1[0], h1[1]};
#pragma unroll
      for (int jj = 0; jj < 8; ++jj) {
        u32x4 a;
#pragma unroll
        for (int e = 0; e < 4; ++e) a[e] = __builtin_amdgcn_perm(wr[st][2 * e + 1][jj >> 1], wr[st][2 * e][jj >> 1], (jj & 1) ? 0x07060302u : 0x05040100u);
#pragma unroll
        for (int rh = 0; rh < 2; ++rh) acc[rh][jj] = MFMA16(asbf(a), asbf(rh ? xa1 : xa0), acc[rh][jj]);
      }
      SBAR();
    };
    qload(0); qload(1); qload(2); qload(3); qload(4); qload(5);
    WAITV(24);
    qstep(0); asm volatile("" ::"v"(cpf)); qload(6);
    BAR();
    qload(7);
    ep = __hip_atomic_load((gu64*)(ws + WS_EP + b * 256), __ATOMIC_RELAXED, __HIP_MEMORY_SCOPE_AGENT);
    SBAR();
    qstep(1); qstep(2); qstep(3);
    f32x4 own[4];
    switch (w) {
      case 0: q_scatter<0>(acc, own, lb + lane * 16); break;
      case 1: q_scatter<1>(acc, own, lb + lane * 16); break;
      case 2: q_scatter<2>(acc, own, lb + lane * 16); break;
      default: q_scatter<3>(acc, own, lb + lane * 16); break;
    }
    BAR();
#pragma unroll
    for (int j = 0; j < 3; ++j) {
      const int src = j < w ? j : j + 1;
      const unsigned pa = lb + lane * 16 + QREG + src * 12288 + (w < src ? w : w - 1) * 4096;
#pragma unroll
      for (int e = 0; e < 4; ++e) own[e] += __builtin_bit_cast(f32x4, ldsr128(pa + e * 1024));
    }
    const unsigned long long tg = ep + TAGC;
    const rsrc_t rqo = mkrs(ws + WS_Q + (b * 32 + h) * 16384, 16384);
#pragma unroll
    for (int reg = 0; reg < 4; ++reg) {
      const u32x4 gr = {cvtpk(own[0][reg] * QSCALE, own[1][reg] * QSCALE), cvtpk(own[2][reg] * QSCALE, own[3][reg] * QSCALE), (unsigned)tg, (unsigned)(tg >> 32)};
      st128(gr, rqo, ((2 * g + (reg >> 1)) * 2 + (w & 1)) * 1024 + ((reg & 1) * 32 + 16 * (w >> 1) + c) * 16);
      *(LAS u32x2*)(size_t)(lb + KRING + 3 * SLOT + ((2 * g + (reg >> 1)) * 64 + (reg & 1) * 32 + 16 * (w >> 1) + c) * 16 + (w & 1) * 8) = u32x2{gr[0], gr[1]};
    }
  }
  const unsigned long long tag = ep + TAGC;
  bf16x8 qf[8];
  {
    BAR();
    __builtin_amdgcn_s_sleep(QDELAY);
    if (w == sp) {
      BAR();
#pragma unroll
      for (int s = 0; s < 8; ++s) qf[s] = asbf(ldsr128(lb + KRING + 3 * SLOT + s * 1024 + lane * 16));
    } else {
      const rsrc_t rqi = mkrs(ws + WS_Q + (b * 32 + hkv * 4 + w) * 16384, 16384);
      u32x4 gq[16];
#pragma unroll
      for (int i = 0; i < 16; ++i) gq[i] = ld128(rqi, lane * 16 + i * 1024, 0);
      BAR();
#pragma unroll 1
      for (unsigned spins = 0; spins < SPIN_CAP; ++spins) {
        bool ok = true;
#pragma unroll
        for (int i = 0; i < 16; ++i) ok &= gq[i][2] == (unsigned)tag && gq[i][3] == (unsigned)(tag >> 32);
        if (__all(ok)) break;
        __builtin_amdgcn_s_sleep(2);
#pragma unroll
        for (int i = 0; i < 16; ++i) gq[i] = ld128(rqi, lane * 16 + i * 1024, 0);
      }
#pragma unroll
      for (int s = 0; s < 8; ++s) qf[s] = asbf(u32x4{gq[2 * s][0], gq[2 * s][1], gq[2 * s + 1][0], gq[2 * s + 1][1]});
    }
  }

  const int r = lane & 31, hh = lane >> 5;
  f32x16 o[4], negm;
#pragma unroll
  for (int i = 0; i < 16; ++i) { o[0][i] = 0.f; o[1][i] = 0.f; o[2][i] = 0.f; o[3][i] = 0.f; negm[i] = 0.f; }
  float m = 0.f;
  const unsigned kbase = lb + KRING + 256 * r + 16 * (hh ^ (r & 15));
  const int q4 = (lane & 15) >> 2, p4 = lane & 3, blk = (lane >> 4) & 1;
  const unsigned vlane = 256 * (4 * hh + q4) + 32 * blk + 8 * p4;
  WAITV(0);
  f32x16 s0, s1;
  u32x4 pf[4];
  const unsigned on1 = (lane == 0 || lane == 32 || lane == 17 || lane == 49) ? 0x3F803F80u : 0u;
  const bf16x8 aone = asbf(u32x4{on1, on1, on1, on1});
  f32x4 lacc = {0.f, 0.f, 0.f, 0.f};
  auto iter = [&](auto first_c, const int t) {
    constexpr bool FIRST = decltype(first_c)::value;
    const unsigned so = (t & 3) * SLOT, sv = ((t - 1) & 3) * SLOT;
    BAR();
    u32x4 kf[16];
    unsigned ka[8];
#pragma unroll
    for (int s = 0; s < 8; ++s) ka[s] = (kbase ^ (s << 5)) + so;
    auto kread = [&](int i) { kf[i] = ldsr128(ka[i & 7] + (i >> 3) * 8192); };
    kread(1); kread(0); kread(3); kread(2); kread(5); kread(4);
    SBAR();
    float e1[16], e0[16];
    auto fin = [&](int e) { e1[e] = __builtin_amdgcn_exp2f(s1[e]); PIN(e1[e]); };
    auto fincvt = [&](int d) { unsigned v = cvtpk(e1[2 * d], e1[2 * d + 1]); PIN(v); pf[2 + (d >> 2)][d & 3] = v; };
    if constexpr (!FIRST) {
#pragma unroll
      for (int e = 0; e < 8; ++e) fin(e);
      fincvt(0); fincvt(1);
      SBAR();
    }
    unsigned va[4];
    bf16x8 vf[16];
    auto vread = [&](int i) { vf[i] = ldstr(va[i & 3] + (i >> 2) * 4096); };
    float pm0 = 0.f;
#pragma unroll
    for (int mi = 0; mi < 16; ++mi) {
      if (mi == 0) s0 = MFMA32(asbf(kf[0]), qf[0], negm);
      else if (mi < 8) s0 = MFMA32(asbf(kf[mi]), qf[mi], s0);
      else if (mi == 8) s1 = MFMA32(asbf(kf[8]), qf[0], negm);
      else s1 = MFMA32(asbf(kf[mi]), qf[mi - 8], s1);
      if ((mi & 1) == 0 && mi + 6 < 16) { kread(mi + 7); kread(mi + 6); }
      if constexpr (!FIRST) {
        if (mi < 4) { fin(8 + 2 * mi); fin(9 + 2 * mi); }
        if (mi < 6) fincvt(2 + mi);
        if (mi == 9) { for (int db = 0; db < 4; ++db) va[db] = lb + VRING + sv + vlane + 64 * (db ^ q4); }
        if (mi == 10) { vread(1); vread(0); }
        if (mi == 12) { vread(3); vread(2); }
        if (mi == 14) { vread(5); vread(4); }
      }
      if (mi == 10) asm volatile("" ::"v"(negm));
      if (mi == 11) { pm0 = fmx3(s0[0], s0[1], s0[2]); pm0 = fmx3(pm0, s0[3], s0[4]); pm0 = fmx3(pm0, s0[5], s0[6]); }
      if (mi == 13) { pm0 = fmx3(pm0, s0[7], s0[8]); pm0 = fmx3(pm0, s0[9], s0[10]); pm0 = fmx3(pm0, s0[11], s0[12]); }
      if (mi == 15) { pm0 = fmx3(pm0, s0[13], s0[14]); pm0 = fmx(pm0, s0[15]); }
      SBAR();
    }
    float pm = pm0, alpha = 1.f;
    bool pend = false;
    auto pvgap = [&](int mi) {
      if constexpr (!FIRST) {
        o[mi & 3] = MFMA32(vf[mi], asbf(pf[mi >> 2]), o[mi & 3]);
        if ((mi & 1) == 0 && mi + 6 < 16) { vread(mi + 7); vread(mi + 6); }
        if ((mi & 3) == 3) lacc = MFMA16(aone, asbf(pf[mi >> 2]), lacc);
      }
    };
    pvgap(0); SBAR();
    pvgap(1); pm = fmx3(pm, s1[0], s1[1]); pm = fmx3(pm, s1[2], s1[3]); pm = fmx3(pm, s1[4], s1[5]); pm = fmx3(pm, s1[6], s1[7]); SBAR();
    pvgap(2); pm = fmx3(pm, s1[8], s1[9]); pm = fmx3(pm, s1[10], s1[11]); pm = fmx3(pm, s1[12], s1[13]); pm = fmx3(pm, s1[14], s1[15]); SBAR();
    pvgap(3);
    { auto rr = __builtin_amdgcn_permlane32_swap(__float_as_uint(pm), __float_as_uint(pm), false, false);
      pm = fmx(__uint_as_float(rr[0]), __uint_as_float(rr[1])); }
    SBAR();
    if (FIRST || __builtin_expect(__any(pm > THR), 0)) {
      const float dl = FIRST ? pm : fmx(pm, 0.f);
      m += dl;
#pragma unroll
      for (int i = 0; i < 16; ++i) { s0[i] -= dl; s1[i] -= dl; negm[i] = -m; }
      if constexpr (!FIRST) { alpha = __builtin_amdgcn_exp2f(-dl); pend = true; }
    }
    SBAR();
#pragma unroll
    for (int mi = 4; mi < 16; ++mi) {
      pvgap(mi);
      if (mi < 12) { const int e = 2 * (mi - 4); e0[e] = __builtin_amdgcn_exp2f(s0[e]); e0[e + 1] = __builtin_amdgcn_exp2f(s0[e + 1]); PIN(e0[e]); PIN(e0[e + 1]); }
      if (mi >= 8 && mi < 12) { unsigned v = cvtpk(e0[2 * (mi - 8)], e0[2 * (mi - 8) + 1]); PIN(v); pf[0][mi - 8] = v; }
      if (mi >= 12) { unsigned v = cvtpk(e0[8 + 2 * (mi - 12)], e0[8 + 2 * (mi - 12) + 1]); PIN(v); pf[1][mi - 12] = v; }
      SBAR();
    }
    if constexpr (!FIRST) {
      if (__builtin_expect(pend, 0)) {
#pragma unroll
        for (int db = 0; db < 4; ++db)
#pragma unroll
          for (int i = 0; i < 16; ++i) o[db][i] *= alpha;
        lacc[0] *= __shfl(alpha, lane & 15);
        lacc[1] *= __shfl(alpha, 16 + (lane & 15));
      }
    }
  };
  iter(std::true_type{}, 0);
#pragma unroll 1
  for (int t = 1; t < 8; ++t) iter(std::false_type{}, t);
  {
    float e1[16];
#pragma unroll
    for (int e = 0; e < 16; ++e) e1[e] = __builtin_amdgcn_exp2f(s1[e]);
#pragma unroll
    for (int d = 0; d < 8; ++d) pf[2 + (d >> 2)][d & 3] = cvtpk(e1[2 * d], e1[2 * d + 1]);
#pragma unroll
    for (int ks = 0; ks < 4; ++ks) lacc = MFMA16(aone, asbf(pf[ks]), lacc);
    asm volatile("" : "+v"(pf[2]), "+v"(pf[3]), "+v"(lacc));
  }
  gu64* const cfp = (gu64*)(ws + WS_FC + ((lane & 7) + 8 * b + 64 * sp) * 256);
  unsigned long long cf = __hip_atomic_load(cfp, __ATOMIC_RELAXED, __HIP_MEMORY_SCOPE_AGENT);
  float l;
  { float la = __shfl(lacc[0], lane & 15), lc = __shfl(lacc[1], lane & 15);
    asm volatile("" : "+v"(la), "+v"(lc));
    l = r < 16 ? la : lc; }
  const int pgrp = (b * 8 + hkv) * 4;
  const rsrc_t rp = mkrs(ws + WS_PART + ((pgrp + sp) * 4 + w) * 8192, 8192);
  BAR();
  if (!__all(cf == tag)) {
#pragma unroll 1
    for (unsigned spins = 0; spins < SPIN_CAP; ++spins) {
      __builtin_amdgcn_s_sleep(2);
      if (__all(__hip_atomic_load(cfp, __ATOMIC_RELAXED, __HIP_MEMORY_SCOPE_AGENT) == tag)) break;
    }
  }
  {
    unsigned va[4];
#pragma unroll
    for (int db = 0; db < 4; ++db) va[db] = lb + VRING + 3 * SLOT + vlane + 64 * (db ^ q4);
    bf16x8 vf[16];
    auto vread = [&](int i) { vf[i] = ldstr(va[i >> 2] + (i & 3) * 4096); };
    auto publish = [&](int db, int sx) {
      u32x4 v;
#pragma unroll
      for (int i = 0; i < 4; ++i) v[i] = cvtpk(o[db][8 * sx + 2 * i], o[db][8 * sx + 2 * i + 1]);
      st128(v, rp, lane * 16 + (2 * db + sx) * 1024);
      if (db >= 2) ldsw128(lb + KRING + 3 * SLOT + w * 4096 + (2 * (db - 2) + sx) * 1024 + lane * 16, v);
    };
    vread(1); vread(0); vread(3); vread(2); vread(5); vread(4);
    SBAR();
#pragma unroll
    for (int mi = 0; mi < 16; ++mi) {
      o[mi >> 2] = MFMA32(vf[mi], asbf(pf[mi & 3]), o[mi >> 2]);
      if ((mi & 1) == 0 && mi + 6 < 16) { vread(mi + 7); vread(mi + 6); }
      if (mi >= 6 && (mi & 3) >= 2) publish((mi >> 2) - 1, mi & 1);
      SBAR();
    }
    publish(3, 0); publish(3, 1);
  }
  BAR();
  WAITV(0);
  if (hh == 0) st128(u32x4{__float_as_uint(m), __float_as_uint(l), (unsigned)tag, (unsigned)(tag >> 32)}, mkrs(ws + WS_ML + ((pgrp + sp) * 4 + w) * 512, 512), r * 16);
  BAR();
  {
    u32x4 st[3], pr[3][4];
#pragma unroll 1
    for (unsigned spins = 0; spins < SPIN_CAP; ++spins) {
      bool ok = true;
#pragma unroll
      for (int j = 0; j < 3; ++j) st[j] = ld128(rws, r * 16, WS_ML + ((pgrp + ((sp + 1 + j) & 3)) * 4 + w) * 512);
#pragma unroll
      for (int j = 0; j < 3; ++j) ok &= st[j][2] == (unsigned)tag && st[j][3] == (unsigned)(tag >> 32);
      if (__all(ok)) break;
      __builtin_amdgcn_s_sleep(HSLEEP);
    }
    asm volatile("" ::: "memory");
#pragma unroll
    for (int j = 0; j < 3; ++j)
#pragma unroll
      for (int f = 0; f < 4; ++f) pr[j][f] = ld128(rws, lane * 16 + f * 1024, WS_PART + ((pgrp + ((sp + 1 + j) & 3)) * 4 + w) * 8192);
    float ms[4], ls[4], a[4];
#pragma unroll
    for (int q = 0; q < 3; ++q) { ms[q] = __uint_as_float(st[q][0]); ls[q] = __uint_as_float(st[q][1]); }
    ms[3] = m; ls[3] = l;
    split_weights(ms, ls, a);
#pragma unroll
    for (int f = 0; f < 4; ++f) {
      u32x4 ov;
#pragma unroll
      for (int i = 0; i < 4; ++i) {
        float lo = a[3] * o[f >> 1][8 * (f & 1) + 2 * i], hi = a[3] * o[f >> 1][8 * (f & 1) + 2 * i + 1];
#pragma unroll
        for (int j = 0; j < 3; ++j) { lo += a[j] * bflo(pr[j][f][i]); hi += a[j] * bfhi(pr[j][f][i]); }
        ov[i] = cvtpk(lo, hi);
      }
      ldsw128(lb + ofrag_lds(w) + f * 1024 + lane * 16, ov);
    }
  }
  BAR();
  {
    const unsigned wl = lb + vlane + 64 * (w ^ q4), ol = lb + lane * 16;
    f32x16 acc, acc2;
#pragma unroll
    for (int i = 0; i < 16; ++i) { acc[i] = 0.f; acc2[i] = 0.f; }
    bf16x8 wf[32]; u32x4 of[32];
    auto rd = [&](int i) { const int kt = i >> 2, ks = i & 3; of[i] = ldsr128(ol + ofrag_lds(kt >> 1) + (4 * (kt & 1) + ks) * 1024); wf[i] = ldstr(wl + wo_lds(kt) + 4096 * ks); };
    rd(1); rd(0); rd(3); rd(2);
    SBAR();
#pragma unroll
    for (int i = 0; i < 32; ++i) {
      if (i & 1) acc2 = MFMA32(wf[i], asbf(of[i]), acc2); else acc = MFMA32(wf[i], asbf(of[i]), acc);
      if ((i & 1) == 0 && i + 4 < 32) { rd(i + 5); rd(i + 4); }
      SBAR();
    }
    acc += acc2;
    const rsrc_t rpo = mkrs(ws + WS_PO + ((b * 4 + sp) * 8 + hkv) * 8192 + w * 2048, 2048);
#pragma unroll
    for (int q2 = 0; q2 < 2; ++q2) {
      u32x4 v;
#pragma unroll
      for (int i = 0; i < 4; ++i) v[i] = notempty(cvtpk(acc[8 * q2 + 2 * i], acc[8 * q2 + 2 * i + 1]));
      st128(v, rpo, lane * 16 + q2 * 1024);
    }
  }
  if (w == 0) {
    __builtin_amdgcn_s_sleep(ODELAY);
    const int pc = lane >> 3, sub = lane & 7, h2 = sub >> 2, row = 4 * hkv + (sub & 3);
    u32x4 pv[8];
#pragma unroll 1
    for (unsigned spins = 0; spins < SPIN_CAP; ++spins) {
#pragma unroll
      for (int k = 0; k < 8; ++k) pv[k] = ld128(rws, pc * 1024 + (h2 * 32 + row) * 16, WS_PO + ((b * 4 + sp) * 8 + k) * 8192);
      unsigned mx = 0;
#pragma unroll
      for (int k = 0; k < 8; ++k) mx = umax4(mx, pv[k]);
      if (__all(mx != EMPTY)) break;
      __builtin_amdgcn_s_sleep(OSLEEP);
    }
    float s[8];
#pragma unroll
    for (int i = 0; i < 4; ++i) { s[2 * i] = bflo(pv[0][i]); s[2 * i + 1] = bfhi(pv[0][i]); }
#pragma unroll
    for (int k = 1; k < 8; ++k)
#pragma unroll
      for (int i = 0; i < 4; ++i) { s[2 * i] += bflo(pv[k][i]); s[2 * i + 1] += bfhi(pv[k][i]); }
    const int n = 32 * (pc >> 1) + 16 * (pc & 1) + 4 * h2;
    const rsrc_t ro = mkrs(out, 262144);
    const unsigned oo = (unsigned)((b * 32 + row) * 512 + sp * 128 + n) * 2;
    __builtin_amdgcn_raw_buffer_store_b64(u32x2{cvtpk(s[0], s[1]), cvtpk(s[2], s[3])}, ro, oo, 0, 0);
    __builtin_amdgcn_raw_buffer_store_b64(u32x2{cvtpk(s[4], s[5]), cvtpk(s[6], s[7])}, ro, oo + 16, 0, 0);
    if (hkv == 0 && sp == 0 && lane == 0) __hip_atomic_store((gu64*)(ws + WS_EP + b * 256), ep + 1ull, __ATOMIC_RELAXED, __HIP_MEMORY_SCOPE_AGENT);
  }
}

extern "C" void kernel_launch(void* const* d_in, const int* in_sizes, int n_in, void* d_out, int out_size, void* d_ws, size_t ws_size, hipStream_t stream) {
  (void)in_sizes; (void)n_in; (void)out_size; (void)ws_size;
  (void)hipFuncSetAttribute((const void*)fused_gqa, hipFuncAttributeMaxDynamicSharedMemorySize, LDS_BYTES);
  fused_gqa<<<256, 512, LDS_BYTES, stream>>>((const unsigned short*)d_in[0], (const unsigned short*)d_in[1], (const unsigned short*)d_in[2],
                                             (const unsigned short*)d_in[3], (const unsigned short*)d_in[4], (unsigned short*)d_out, (char*)d_ws);
}
```
